```python
import torch

def _softmax(x, axis=-1):
    return torch.softmax(x, dim=axis)
def _gelu(x):
    return torch.nn.functional.gelu(x, approximate="tanh")
def _silu(x):
    return torch.nn.functional.silu(x)
def _sigmoid(x):
    return torch.sigmoid(x)
def _layernorm(x, axis=-1, eps=1e-5):
    m = x.mean(dim=axis, keepdim=True)
    v = x.var(dim=axis, keepdim=True, unbiased=False)
    return (x - m) / torch.sqrt(v + eps)
def _scatter_add(tgt, idx, src, axis=0):
    return tgt.clone().index_add_(axis, idx, src)
def _segment_sum(src, seg, n):
    out = torch.zeros((n,) + src.shape[1:], dtype=src.dtype)
    return out.index_add_(0, seg, src)
def _pad(x, pw):
    flat = tuple(v for pair in reversed(pw) for v in pair)
    return torch.nn.functional.pad(x, flat)

kernel_name = 'parallel_sum__mha_block__glu_gated_reglu__prompt'

def setup_inputs():

    _w = (lambda a: (lambda t: t.to(torch.bfloat16) if t.is_floating_point() else t)(torch.as_tensor(a)))
    def _A_setup():
        B, S, D, H = 1, 2048, 640, 32
        return {
            "x": _w((lambda *s: torch.randn(*s, dtype=torch.bfloat16) * 0.05)(B, S, D)),
            "wqkv": _w((lambda *s: torch.randn(*s, dtype=torch.bfloat16) * 0.05)(D, 3 * D)),
            "wo": _w((lambda *s: torch.randn(*s, dtype=torch.bfloat16) * 0.05)(D, D)),
        }
    _A_in = _A_setup()

    def _B_setup():
        B, S, D, F = 1, 2048, 640, 1536
        return {
            "x": _w((lambda *s: torch.randn(*s, dtype=torch.bfloat16) * 0.05)(B, S, D)),
            "W_gate": _w((lambda *s: torch.randn(*s, dtype=torch.bfloat16) * 0.4)(D, F) * (2.0 / D) ** 0.5),
            "W_up": _w((lambda *s: torch.randn(*s, dtype=torch.bfloat16) * 0.4)(D, F) * (2.0 / D) ** 0.5),
            "W_down": _w((lambda *s: torch.randn(*s, dtype=torch.bfloat16) * 0.4)(F, D) * (1.0 / F) ** 0.5),
        }
    _B_in = _B_setup()

    return {
        "A_x": _A_in["x"],
        "A_wqkv": _A_in["wqkv"],
        "A_wo": _A_in["wo"],
        "B_x": _B_in["x"],
        "B_W_gate": _B_in["W_gate"],
        "B_W_up": _B_in["W_up"],
        "B_W_down": _B_in["W_down"],
    }

def reference(A_x, A_wqkv, A_wo, B_x, B_W_gate, B_W_up, B_W_down):
    def _A_ref(x, wqkv, wo):
        B, S, D = x.shape
        H = 32
        Dh = D // H
        qkv = (lambda x, *d: x.permute(*d))((x @ wqkv).reshape(B, S, 3, H, Dh), 2, 0, 3, 1, 4)
        q, k, v = qkv[0], qkv[1], qkv[2]
        scores = torch.einsum("bhqd,bhkd->bhqk", q, k) * (Dh ** -0.5)
        p = _softmax(scores, axis=-1)
        o = torch.einsum("bhqk,bhkd->bhqd", p, v)
        return (lambda x, *d: x.permute(*d))(o, 0, 2, 1, 3).reshape(B, S, D) @ wo

    def _B_ref(x, W_gate, W_up, W_down):
        gate = torch.einsum("bsd,df->bsf", x, W_gate)
        up = torch.einsum("bsd,df->bsf", x, W_up)
        gate = gate * (lambda x, dt: x.to(dt))(gate > 0, gate.dtype)
        h = gate * up
        return torch.einsum("bsf,fd->bsd", h, W_down)

    x = A_x
    xn = _layernorm(x, axis=-1)
    return x + _A_ref(xn, A_wqkv, A_wo) + _B_ref(xn, B_W_gate, B_W_up, B_W_down)
```

```cpp
#include <hip/hip_runtime.h>
#include <hip/hip_bf16.h>
typedef __hip_bfloat16 bf16;
constexpr int S=2048, D=640, H=32, DH=20, F=1536;
__device__ inline float bf(float x){ return __bfloat162float(__float2bfloat16(x)); }
__global__ void ln_k(const bf16* x, float* xn){
  int row=blockIdx.x; __shared__ float red[256];
  float s=0; for(int i=threadIdx.x;i<D;i+=256) s+=__bfloat162float(x[row*D+i]);
  red[threadIdx.x]=s; __syncthreads(); for(int o=128;o>0;o>>=1){ if(threadIdx.x<o) red[threadIdx.x]+=red[threadIdx.x+o]; __syncthreads(); }
  float mean=red[0]/D; __syncthreads();
  float v=0; for(int i=threadIdx.x;i<D;i+=256){ float d=__bfloat162float(x[row*D+i])-mean; v+=d*d; }
  red[threadIdx.x]=v; __syncthreads(); for(int o=128;o>0;o>>=1){ if(threadIdx.x<o) red[threadIdx.x]+=red[threadIdx.x+o]; __syncthreads(); }
  float var=bf(red[0]/D); float mb=bf(mean); float den=bf(sqrtf(bf(var+1e-5f)));
  for(int i=threadIdx.x;i<D;i+=256) xn[row*D+i]=bf(bf(__bfloat162float(x[row*D+i])-mb)/den);
}
__global__ void gemm_k(const float* A, const bf16* W, float* C, int M,int N,int K){
  int n=blockIdx.x*64+threadIdx.x%64, m=blockIdx.y*4+threadIdx.x/64;
  float acc=0; for(int k=0;k<K;++k) acc+=A[m*K+k]*__bfloat162float(W[k*N+n]);
  C[m*N+n]=bf(acc);
}
__global__ void attn_k(const float* qkv, float* o){
  int h=blockIdx.y, q=blockIdx.x*64+threadIdx.x;
  float qq[DH]; for(int d=0;d<DH;++d) qq[d]=qkv[q*3*D+h*DH+d];
  float m=-1e30f,l=0,acc[DH]; for(int d=0;d<DH;++d) acc[d]=0;
  for(int k=0;k<S;++k){ float s=0; for(int d=0;d<DH;++d) s+=qq[d]*qkv[k*3*D+D+h*DH+d]; s*=0.22360679775f;
    float mn=fmaxf(m,s); float a=__expf(m-mn), p=__expf(s-mn); l=l*a+p; for(int d=0;d<DH;++d) acc[d]=acc[d]*a+p*qkv[k*3*D+2*D+h*DH+d]; m=mn; }
  for(int d=0;d<DH;++d) o[q*D+h*DH+d]=bf(acc[d]/l);
}
__global__ void glu_k(const float* g,const float* u,float* h){ int i=blockIdx.x*256+threadIdx.x; float a=g[i]; h[i]=bf((a>0?a:0.f)*u[i]); }
__global__ void fin_k(const bf16* x,const float* a,const float* b,bf16* out){ int i=blockIdx.x*256+threadIdx.x; out[i]=__float2bfloat16(bf(__bfloat162float(x[i])+a[i])+b[i]); }
extern "C" void kernel_launch(void* const* d_in, const int* in_sizes, int n_in, void* d_out, int out_size, void* d_ws, size_t ws_size, hipStream_t stream) {
  const bf16* x=(const bf16*)d_in[0]; const bf16* wqkv=(const bf16*)d_in[1]; const bf16* wo=(const bf16*)d_in[2];
  const bf16* wg=(const bf16*)d_in[4]; const bf16* wu=(const bf16*)d_in[5]; const bf16* wd=(const bf16*)d_in[6];
  float* ws=(float*)d_ws; float* xn=ws; float* qkv=xn+S*D; float* o=qkv+S*3*D; float* g=o+S*D; float* u=g+S*F; float* hh=u+S*F; float* a=hh+S*F; float* b=a+S*D;
  ln_k<<<S,256,0,stream>>>(x,xn);
  gemm_k<<<dim3(3*D/64,S/4),256,0,stream>>>(xn,wqkv,qkv,S,3*D,D);
  attn_k<<<dim3(S/64,H),64,0,stream>>>(qkv,o);
  gemm_k<<<dim3(D/64,S/4),256,0,stream>>>(o,wo,a,S,D,D);
  gemm_k<<<dim3(F/64,S/4),256,0,stream>>>(xn,wg,g,S,F,D);
  gemm_k<<<dim3(F/64,S/4),256,0,stream>>>(xn,wu,u,S,F,D);
  glu_k<<<S*F/256,256,0,stream>>>(g,u,hh);
  gemm_k<<<dim3(D/64,S/4),256,0,stream>>>(hh,wd,b,S,D,F);
  fin_k<<<S*D/256,256,0,stream>>>(x,a,b,(bf16*)d_out);
}
```

```cpp
#include <hip/hip_runtime.h>
#include <hip/hip_bf16.h>
#include <stdint.h>

#define DI __device__ __forceinline__
#define LAS __attribute__((address_space(3)))
typedef short bf16x8 __attribute__((ext_vector_type(8)));
typedef short s16x4 __attribute__((ext_vector_type(4)));
typedef float f32x4 __attribute__((ext_vector_type(4)));
typedef float f32x2 __attribute__((ext_vector_type(2)));
typedef float f32x16 __attribute__((ext_vector_type(16)));
typedef unsigned u32x4 __attribute__((ext_vector_type(4)));
typedef unsigned u32x2 __attribute__((ext_vector_type(2)));
typedef __bf16 bf16x2_t __attribute__((ext_vector_type(2)));
typedef LAS char* lds_ptr;

constexpr int S = 2048, D = 640, H = 32, DH = 20, F = 1536;
constexpr size_t QGS = 5120, QHS = 16 * QGS;
constexpr size_t KVGS = 10496, KVHS = 16 * KVGS;
constexpr size_t WS_Q = 0;
constexpr size_t WS_KV = WS_Q + 32 * QHS;
constexpr size_t WS_XN = WS_KV + 32 * KVHS + 4096;
constexpr size_t WS_H = WS_XN + (size_t)S * D * 2;
constexpr size_t WS_O = WS_H + (size_t)S * F * 2;
constexpr size_t WS_KP = WS_O + (size_t)S * D * 2;

#define SBAR() __builtin_amdgcn_sched_barrier(0)
#define MFMA16(a, b, c) __builtin_amdgcn_mfma_f32_16x16x32_bf16(a, b, c, 0, 0, 0)
#define MFMA32(a, b, c) __builtin_amdgcn_mfma_f32_32x32x16_bf16(a, b, c, 0, 0, 0)
#define MFMA32K8(a, b, c) __builtin_amdgcn_mfma_f32_32x32x8bf16_1k(a, b, c, 0, 0, 0)

DI unsigned rfl(unsigned x) { return __builtin_amdgcn_readfirstlane(x); }
DI unsigned cvtpk(float lo, float hi) { f32x2 v = {lo, hi}; bf16x2_t b = __builtin_convertvector(v, bf16x2_t); return __builtin_bit_cast(unsigned, b); }
DI float bfr(float x) { return __uint_as_float(cvtpk(x, 0.f) << 16); }
DI float bflo(unsigned u) { return __uint_as_float(u << 16); }
DI float bfhi(unsigned u) { return __uint_as_float(u & 0xffff0000u); }
DI bf16x8 lds128(lds_ptr p) { return *(const LAS bf16x8*)p; }
DI s16x4 ldstr(lds_ptr p) { return __builtin_amdgcn_ds_read_tr16_b64_v4i16((LAS s16x4*)p); }
DI bf16x8 cat8(s16x4 a, s16x4 b) { return __builtin_shufflevector(a, b, 0, 1, 2, 3, 4, 5, 6, 7); }

DI void glds4(unsigned v0, unsigned v1, unsigned v2, unsigned v3, const void* sbase, unsigned m0v) {
  unsigned sv;
  asm volatile("s_mov_b32 %0, m0\n\ts_mov_b32 m0, %6\n\ts_nop 0\n\t"
               "global_load_lds_dwordx4 %1, %5\n\t"
               "global_load_lds_dwordx4 %2, %5 offset:1024\n\t"
               "global_load_lds_dwordx4 %3, %5 offset:2048\n\t"
               "global_load_lds_dwordx4 %4, %5 offset:3072\n\t"
               "s_mov_b32 m0, %0"
               : "=&s"(sv) : "v"(v0), "v"(v1), "v"(v2), "v"(v3), "s"(sbase), "s"(m0v) : "memory");
}
DI void glds2(unsigned v0, unsigned v1, const void* sbase, unsigned m0v) {
  unsigned sv;
  asm volatile("s_mov_b32 %0, m0\n\ts_mov_b32 m0, %4\n\ts_nop 0\n\t"
               "global_load_lds_dwordx4 %1, %3\n\t"
               "global_load_lds_dwordx4 %2, %3 offset:1024\n\t"
               "s_mov_b32 m0, %0"
               : "=&s"(sv) : "v"(v0), "v"(v1), "s"(sbase), "s"(m0v) : "memory");
}
DI void glds1(unsigned v0, const void* sbase, unsigned m0v) {
  unsigned sv;
  asm volatile("s_mov_b32 %0, m0\n\ts_mov_b32 m0, %3\n\ts_nop 0\n\t"
               "global_load_lds_dwordx4 %1, %2\n\t"
               "s_mov_b32 m0, %0"
               : "=&s"(sv) : "v"(v0), "s"(sbase), "s"(m0v) : "memory");
}
DI void glds1_half(unsigned v0, const void* sbase, unsigned m0v) {
  unsigned sv;
  asm volatile("s_mov_b32 %0, m0\n\ts_mov_b32 m0, %3\n\ts_mov_b32 exec_hi, 0\n\t"
               "global_load_lds_dwordx4 %1, %2\n\t"
               "s_mov_b32 exec_hi, -1\n\ts_mov_b32 m0, %0"
               : "=&s"(sv) : "v"(v0), "s"(sbase), "s"(m0v) : "memory");
}
template <int N> DI void wait_vm_bar() { asm volatile("s_waitcnt vmcnt(%0) lgkmcnt(0)\n\ts_barrier" ::"i"(N) : "memory"); }
DI void wait_lgkm_bar() { asm volatile("s_waitcnt lgkmcnt(0)\n\ts_barrier" ::: "memory"); }

DI f32x2 ln_consts(float sum, float sq) {
  const float mean = sum * (1.0f / D);
  const float var = fmaxf(sq * (1.0f / D) - mean * mean, 0.f);
  const float den = bfr(sqrtf(bfr(bfr(var) + 1e-5f)));
  f32x2 r = {bfr(mean), 1.0f / den};
  return r;
}

constexpr int K1_X = 0, K1_W = 65536, K1_ST = 131072, K1_LDS = K1_ST + 128 * 8 + 128 * 4;

template <bool SWAP>
DI void k1_compute(lds_ptr lds, int wid, int lane, int rb, int cb, char* ws) {
  const int wm = wid >> 1, wn = wid & 1, r = lane & 15, g = lane >> 4;
  lds_ptr xa[2][2];
#pragma unroll
  for (int ks = 0; ks < 2; ++ks)
#pragma unroll
    for (int hf = 0; hf < 2; ++hf)
      xa[ks][hf] = lds + K1_X + (wm * 64 + ((hf ^ wn) * 2) * 16 + r) * 128 + (((4 * ks + g) ^ ((r >> 1) & 7)) * 16);
  lds_ptr wa[4];
  {
    const int q = r >> 2, p = r & 3, f = q | ((g & 1) << 2);
#pragma unroll
    for (int nt = 0; nt < 4; ++nt) wa[nt] = lds + K1_W + (8 * g + q) * 256 + (((wn * 4 + (nt ^ (2 * wm))) ^ f) * 32) + p * 8;
  }
  f32x4 acc[4][4];
#pragma unroll
  for (int i = 0; i < 4; ++i)
#pragma unroll
    for (int j = 0; j < 4; ++j) acc[i][j] = f32x4{0.f, 0.f, 0.f, 0.f};
  f32x4 ssum[2] = {}, ssq[2] = {}, scs[2] = {};
  const bf16x8 ones = {0x3f80, 0x3f80, 0x3f80, 0x3f80, 0x3f80, 0x3f80, 0x3f80, 0x3f80};
  bf16x8 xf[2][4], wf[2][4];
#define K1_READ(B, ST, KS)                                                                                          \
  do {                                                                                                              \
    _Pragma("unroll") for (int mt = 0; mt < 4; ++mt) xf[B][mt] = lds128(xa[KS][mt >> 1] + ((ST) & 3) * 16384 + (mt & 1) * 2048); \
    _Pragma("unroll") for (int nt = 0; nt < 4; ++nt)                                                                \
      wf[B][nt] = cat8(ldstr(wa[nt] + ((ST) & 3) * 16384 + (KS) * 8192), ldstr(wa[nt] + ((ST) & 3) * 16384 + (KS) * 8192 + 1024)); \
  } while (0)
#define K1_MMA(B)                                                                                                   \
  do {                                                                                                              \
    _Pragma("unroll") for (int nt = 0; nt < 4; ++nt)                                                                \
      _Pragma("unroll") for (int mt = 0; mt < 4; ++mt)                                                              \
        acc[nt][mt] = SWAP ? MFMA16(wf[B][nt], xf[B][mt], acc[nt][mt]) : MFMA16(xf[B][mt], wf[B][nt], acc[nt][mt]); \
    _Pragma("unroll") for (int i = 0; i < 2; ++i) {                                                                 \
      ssum[i] = MFMA16(ones, xf[B][i], ssum[i]);                                                                    \
      ssq[i] = MFMA16(xf[B][i], xf[B][i], ssq[i]);                                                                  \
      scs[i] = MFMA16(wf[B][i], ones, scs[i]);                                                                      \
    }                                                                                                               \
  } while (0)
  wait_lgkm_bar();
  K1_READ(0, 0, 0);
#pragma unroll
  for (int st = 0; st < 10; ++st) {
    K1_READ(1, st, 1);
    SBAR();
    K1_MMA(0);
    SBAR();
    if (st < 9) {
      wait_lgkm_bar();
      K1_READ(0, st + 1, 0);
      SBAR();
    }
    K1_MMA(1);
    SBAR();
  }
#undef K1_READ
#undef K1_MMA
  LAS f32x2* stat = (LAS f32x2*)(lds + K1_ST);
  LAS float* csum = (LAS float*)(lds + K1_ST + 1024);
#pragma unroll
  for (int i = 0; i < 2; ++i) {
    const int e = r & 3;
    const float sq = e == 0 ? ssq[i][0] : e == 1 ? ssq[i][1] : e == 2 ? ssq[i][2] : ssq[i][3];
    if ((r >> 2) == g) stat[wm * 64 + (i ^ (2 * wn)) * 16 + r] = ln_consts(ssum[i][0], sq);
    if (r == 0) *(LAS f32x4*)(csum + wn * 64 + (i ^ (2 * wm)) * 16 + 4 * g) = scs[i];
  }
  wait_lgkm_bar();
  const int kind = cb / 5, fb = (cb - kind * 5) * 128 + wn * 64;
  if (SWAP) {
    f32x2 st4[4];
    const float qs = kind == 0 ? 0.22360679774997896f * 1.4426950408889634f : 1.0f;
#pragma unroll
    for (int mt = 0; mt < 4; ++mt) { st4[mt] = stat[wm * 64 + (mt ^ (2 * wn)) * 16 + r]; st4[mt][1] *= qs; }
    char* img = kind == 0 ? ws + WS_Q + (size_t)rb * QGS : ws + WS_KV + (size_t)rb * KVGS;
    const unsigned hs = kind == 0 ? (unsigned)QHS : (unsigned)KVHS;
#pragma unroll
    for (int nt = 0; nt < 4; ++nt) {
      const int nl = (nt ^ (2 * wm)) * 16 + 4 * g;
      const f32x4 cs = *(const LAS f32x4*)(csum + wn * 64 + nl);
      const int f0 = fb + nl, head = f0 / 20, d0 = f0 - head * 20;
      const bool isb = d0 == 16;
      const unsigned base = head * hs + (isb ? 4096 + wm * 512 : wm * 2048 + (d0 >> 3) * 512 + ((d0 >> 2) & 1) * 8) + r * 16;
      const unsigned ts = isb ? 8 : 1024;
      float nmax = 0.f;
#pragma unroll
      for (int mt = 0; mt < 4; ++mt) {
        const int ma = mt ^ (2 * wn);
        float v[4];
#pragma unroll
        for (int e = 0; e < 4; ++e) v[e] = (acc[nt][mt][e] - st4[mt][0] * cs[e]) * st4[mt][1];
        nmax = fmaxf(nmax, v[0] * v[0] + v[1] * v[1] + v[2] * v[2] + v[3] * v[3]);
        u32x2 o = {cvtpk(v[0], v[1]), cvtpk(v[2], v[3])};
        *(u32x2*)(img + base + (ma & 1) * 256 + (ma >> 1) * ts) = o;
      }
      if (kind == 1) {
        nmax = fmaxf(nmax, __shfl_xor(nmax, 1));
        nmax = fmaxf(nmax, __shfl_xor(nmax, 2));
        nmax = fmaxf(nmax, __shfl_xor(nmax, 4));
        nmax = fmaxf(nmax, __shfl_xor(nmax, 8));
        if (r == 0) ((float*)(ws + WS_KP))[(head * 32 + rb * 2 + wm) * 8 + (d0 >> 2)] = nmax;
      }
    }
  } else {
    char* img = ws + WS_KV + (size_t)rb * KVGS + 5120 + wm * 4 * 672 + (g & 1) * 336 + (g >> 1) * 8;
    unsigned off[4]; float cs[4];
#pragma unroll
    for (int nt = 0; nt < 4; ++nt) {
      const int nl = (nt ^ (2 * wm)) * 16 + r, f = fb + nl, head = f / 20, d = f - head * 20;
      cs[nt] = csum[wn * 64 + nl];
      off[nt] = head * (unsigned)KVHS + d * 16;
    }
#pragma unroll
    for (int mt = 0; mt < 4; ++mt) {
      const int ma = mt ^ (2 * wn);
      const f32x4 s01 = *(const LAS f32x4*)(stat + wm * 64 + ma * 16 + 4 * g);
      const f32x4 s23 = *(const LAS f32x4*)(stat + wm * 64 + ma * 16 + 4 * g + 2);
#pragma unroll
      for (int nt = 0; nt < 4; ++nt) {
        const float v0 = (acc[nt][mt][0] - s01[0] * cs[nt]) * s01[1], v1 = (acc[nt][mt][1] - s01[2] * cs[nt]) * s01[3];
        const float v2 = (acc[nt][mt][2] - s23[0] * cs[nt]) * s23[1], v3 = (acc[nt][mt][3] - s23[2] * cs[nt]) * s23[3];
        u32x2 o = {cvtpk(v0, v1), cvtpk(v2, v3)};
        *(u32x2*)(img + off[nt] + ma * 672) = o;
      }
    }
  }
}

__global__ __launch_bounds__(512, 2) void k1_qkv(const char* __restrict__ x, const char* __restrict__ wqkv, char* __restrict__ ws) {
  __shared__ __attribute__((aligned(16))) char smem[K1_LDS];
  lds_ptr lds = (lds_ptr)smem;
  const int lane = threadIdx.x & 63, wid = rfl(threadIdx.x >> 6);
  const int bid = blockIdx.x, xcd = bid & 7, idx = bid >> 3;
  const int rb = xcd * 2 + (idx & 1), cb = idx >> 1;
  if (wid < 4) {
    if (cb < 10) k1_compute<true>(lds, wid, lane, rb, cb, ws);
    else k1_compute<false>(lds, wid, lane, rb, cb, ws);
  } else {
    const int lw = wid - 4;
    unsigned vx[4], vw[4];
#pragma unroll
    for (int i = 0; i < 4; ++i) {
      const int row = 8 * (lw * 4 + i) + (lane >> 3), ch = (lane & 7) ^ ((4 * i + (lane >> 4)) & 7);
      vx[i] = row * 1280 + ch * 16 - 1024 * i;
      const int k = 4 * (lw * 4 + i) + (lane >> 4), f = (lane >> 4) | ((i >> 1) << 2), cw = (lane & 15) ^ (f << 1);
      vw[i] = k * 3840 + cw * 16 - 1024 * i;
    }
    const char* xb = x + (size_t)rb * 128 * 1280;
    const char* wb = wqkv + cb * 256;
    const unsigned l0 = (unsigned)(size_t)lds + lw * 4096;
    int kt = cb + 1; kt = kt >= 10 ? kt - 10 : kt;
#define K1_ISSUE(ST)                                                                  \
  do {                                                                                \
    glds4(vx[0], vx[1], vx[2], vx[3], xb + kt * 128, l0 + K1_X + ((ST) & 3) * 16384); \
    glds4(vw[0], vw[1], vw[2], vw[3], wb + (size_t)kt * (64 * 3840), l0 + K1_W + ((ST) & 3) * 16384); \
    kt = kt == 9 ? 0 : kt + 1;                                                        \
  } while (0)
    K1_ISSUE(0); K1_ISSUE(1); K1_ISSUE(2); K1_ISSUE(3);
    wait_vm_bar<24>();
    wait_vm_bar<16>(); K1_ISSUE(4);
    wait_vm_bar<16>(); K1_ISSUE(5);
    wait_vm_bar<16>(); K1_ISSUE(6);
    wait_vm_bar<16>(); K1_ISSUE(7);
    wait_vm_bar<16>(); K1_ISSUE(8);
    wait_vm_bar<16>(); K1_ISSUE(9);
    wait_vm_bar<16>();
    wait_vm_bar<8>();
    wait_vm_bar<0>();
#undef K1_ISSUE
    wait_lgkm_bar();
    const int r = lane & 15, g = lane >> 4;
    if (cb < 10) {
      const LAS f32x2* stat = (const LAS f32x2*)(lds + K1_ST);
#pragma unroll
      for (int i = 0; i < 2; ++i) {
        const int mt = lw * 2 + i;
        const f32x2 st = stat[mt * 16 + r];
#pragma unroll
        for (int ks = 0; ks < 2; ++ks) {
          const u32x4 v = __builtin_bit_cast(u32x4, lds128(lds + K1_X + 16384 + (mt * 16 + r) * 128 + (((4 * ks + g) ^ ((r >> 1) & 7)) * 16)));
          u32x4 o;
#pragma unroll
          for (int e = 0; e < 4; ++e) {
            const unsigned t = cvtpk(bflo(v[e]) - st[0], bfhi(v[e]) - st[0]);
            o[e] = cvtpk(bflo(t) * st[1], bfhi(t) * st[1]);
          }
          *(u32x4*)(ws + WS_XN + ((size_t)((rb * 8 + mt) * 80 + cb * 8 + 4 * ks) * 16) * 16 + lane * 16) = o;
        }
      }
    } else if (cb == 10) {
      const u32x4 o = {0x3f803f80u, 0x3f803f80u, 0x3f803f80u, 0x3f803f80u};
#pragma unroll
      for (int i = 0; i < 2; ++i) {
        const int u = (lw * 2 + i) * 64 + lane, head = u >> 4, j = u & 15;
        *(u32x4*)(ws + WS_KV + (size_t)head * KVHS + (size_t)rb * KVGS + 5120 + j * 336 + 320) = o;
      }
    }
  }
}

constexpr int KG_WSUB = 64 * 192 + 32, KG_WST = 24832;
constexpr int KG_X = 0, KG_W = 3 * 16384, KG_LDS = KG_W + 3 * KG_WST;

__global__ __launch_bounds__(512, 2) void kg_gateup(const char* __restrict__ wg, const char* __restrict__ wu, char* __restrict__ ws) {
  __shared__ __attribute__((aligned(16))) char smem[KG_LDS];
  lds_ptr lds = (lds_ptr)smem;
  const int lane = threadIdx.x & 63, wid = rfl(threadIdx.x >> 6);
  const int bid = blockIdx.x, xcd = bid & 7, idx = bid >> 3;
  const int rb = (xcd & 1) * 8 + (idx & 7), fbk = (xcd >> 1) * 4 + (idx >> 3);
  if (wid < 4) {
    const int wm = wid >> 1, wn = wid & 1, r = lane & 31, hh = lane >> 5;
    lds_ptr xa = lds + KG_X + (wm * 4 + (r >> 4)) * 2048 + hh * 256 + (r & 15) * 16;
    lds_ptr wa = lds + KG_W + ((lane >> 4) & 1) * KG_WSUB + (8 * hh + ((lane & 15) >> 2)) * 192 + wn * 96 + (lane & 3) * 8;
    f32x16 acc[3][2];
#pragma unroll
    for (int i = 0; i < 3; ++i)
#pragma unroll
      for (int j = 0; j < 2; ++j) acc[i][j] = f32x16{};
    bf16x8 xf[2][2][2], wf[2][2][3];
#define KG_READ(B, ST, HALF)                                                                                        \
  do {                                                                                                              \
    _Pragma("unroll") for (int s = 0; s < 2; ++s) {                                                                 \
      _Pragma("unroll") for (int mt = 0; mt < 2; ++mt) xf[B][s][mt] = lds128(xa + ((ST) % 3) * 16384 + mt * 4096 + ((HALF) * 2 + s) * 512); \
      _Pragma("unroll") for (int nt = 0; nt < 3; ++nt)                                                              \
        wf[B][s][nt] = cat8(ldstr(wa + ((ST) % 3) * KG_WST + ((HALF) * 2 + s) * 16 * 192 + nt * 32),               \
                            ldstr(wa + ((ST) % 3) * KG_WST + (((HALF) * 2 + s) * 16 + 4) * 192 + nt * 32));        \
    }                                                                                                               \
  } while (0)
#define KG_MMA(B)                                                                                                   \
  do {                                                                                                              \
    _Pragma("unroll") for (int s = 0; s < 2; ++s)                                                                   \
      _Pragma("unroll") for (int nt = 0; nt < 3; ++nt)                                                              \
        _Pragma("unroll") for (int mt = 0; mt < 2; ++mt) acc[nt][mt] = MFMA32(wf[B][s][nt], xf[B][s][mt], acc[nt][mt]); \
  } while (0)
    wait_lgkm_bar();
    KG_READ(0, 0, 0);
#pragma unroll
    for (int st = 0; st < 10; ++st) {
      KG_READ(1, st, 1);
      SBAR();
      KG_MMA(0);
      SBAR();
      if (st < 9) {
        wait_lgkm_bar();
        KG_READ(0, st + 1, 0);
        SBAR();
      }
      KG_MMA(1);
      SBAR();
    }
#undef KG_READ
#undef KG_MMA
#pragma unroll
    for (int mt = 0; mt < 2; ++mt) {
      const int tok = rb * 128 + wm * 64 + mt * 32 + r;
      char* hp = ws + WS_H + ((size_t)((tok >> 4) * 192 + fbk * 12 + wn * 6 + hh) * 16 + (tok & 15)) * 16;
#pragma unroll
      for (int nt = 0; nt < 3; ++nt) {
        unsigned w[4];
#pragma unroll
        for (int e = 0; e < 4; ++e) {
          const unsigned gg = cvtpk(acc[nt][mt][2 * e], acc[nt][mt][2 * e + 1]), uu = cvtpk(acc[nt][mt][8 + 2 * e], acc[nt][mt][8 + 2 * e + 1]);
          const float g0 = bflo(gg), g1 = bfhi(gg);
          w[e] = cvtpk((g0 > 0.f ? g0 : g0 * 0.f) * bflo(uu), (g1 > 0.f ? g1 : g1 * 0.f) * bfhi(uu));
        }
        auto s0 = __builtin_amdgcn_permlane32_swap(w[0], w[2], false, false);
        auto s1 = __builtin_amdgcn_permlane32_swap(w[1], w[3], false, false);
        u32x4 o = {s0[0], s1[0], s0[1], s1[1]};
        *(u32x4*)(hp + nt * 512) = o;
      }
    }
  } else {
    const int lw = wid - 4;
    const unsigned vx0 = lane * 16, vx2 = lane * 16 + 80 * 256 - 2048;
    const char* xb = ws + WS_XN + (size_t)(rb * 8 + lw * 2) * 80 * 256;
    unsigned vw[6];
#pragma unroll
    for (int j = 0; j < 6; ++j) {
      const int u = j * 64 + lane, k = u / 12, pos = u - k * 12;
      vw[j] = k * 3072 + pos * 16 - 1024 * (j & 3);
    }
    const char* wb = ((lw >> 1) ? wu : wg) + fbk * 192 + (size_t)(lw & 1) * 32 * 3072;
    const unsigned lx = (unsigned)(size_t)lds + KG_X + lw * 4096;
    const unsigned lwb = (unsigned)(size_t)lds + KG_W + (lw >> 1) * KG_WSUB + (lw & 1) * 32 * 192;
#define KG_ISSUE(ST)                                                                         \
  do {                                                                                       \
    glds4(vx0, vx0, vx2, vx2, xb + (ST) * 2048, lx + ((ST) % 3) * 16384);                    \
    glds4(vw[0], vw[1], vw[2], vw[3], wb + (size_t)(ST) * (64 * 3072), lwb + ((ST) % 3) * KG_WST); \
    glds2(vw[4], vw[5], wb + (size_t)(ST) * (64 * 3072), lwb + ((ST) % 3) * KG_WST + 4096);  \
  } while (0)
    KG_ISSUE(0); KG_ISSUE(1); KG_ISSUE(2);
    wait_vm_bar<20>();
    wait_vm_bar<10>(); KG_ISSUE(3);
    wait_vm_bar<10>(); KG_ISSUE(4);
    wait_vm_bar<10>(); KG_ISSUE(5);
    wait_vm_bar<10>(); KG_ISSUE(6);
    wait_vm_bar<10>(); KG_ISSUE(7);
    wait_vm_bar<10>(); KG_ISSUE(8);
    wait_vm_bar<10>(); KG_ISSUE(9);
    wait_vm_bar<10>();
    wait_vm_bar<0>();
#undef KG_ISSUE
  }
}

constexpr int K2_CH = 2 * (int)KVGS;
constexpr int K2_SLOT = K2_CH, K2_FLAG = 3 * K2_SLOT, K2_LDS = K2_FLAG + 16;

__global__ __launch_bounds__(512, 2) void k2_attn(char* __restrict__ ws) {
  __shared__ __attribute__((aligned(16))) char smem[K2_LDS];
  lds_ptr lds = (lds_ptr)smem;
  const int lane = threadIdx.x & 63, wid = rfl(threadIdx.x >> 6);
  const int bid = blockIdx.x, xcd = bid & 7, idx = bid >> 3;
  const int head = xcd * 4 + (idx >> 3), qb = idx & 7;
  const int wq = wid & 3, kh = wid >> 2, r = lane & 31, hh = lane >> 5;
  const char* kvg = ws + WS_KV + (size_t)head * KVHS;
  const unsigned ldsb = (unsigned)(size_t)lds;
  const unsigned vl = lane * 16;
#define K2_ISSUE(C, SLOT)                                                                     \
  do {                                                                                        \
    const char* src_ = kvg + (size_t)(C) * K2_CH + wid * 1024;                                \
    const unsigned dst_ = ldsb + (SLOT) + wid * 1024;                                         \
    glds1(vl, src_, dst_);                                                                    \
    glds1(vl, src_ + 8192, dst_ + 8192);                                                      \
    if (wid < 4) glds1(vl, src_ + 16384, dst_ + 16384);                                       \
    else if (wid == 4) glds1_half(vl, src_ + 16384, dst_ + 16384);                            \
  } while (0)
  K2_ISSUE(0, 0);
  K2_ISSUE(1, K2_SLOT);
  const char* qg = ws + WS_Q + (size_t)head * QHS + (size_t)(qb * 2 + (wq >> 1)) * QGS;
  bf16x8 qa[2];
  qa[0] = *(const bf16x8*)(qg + ((wq & 1) * 2) * 1024 + lane * 16);
  qa[1] = *(const bf16x8*)(qg + ((wq & 1) * 2 + 1) * 1024 + lane * 16);
  const u32x4 qbb = *(const u32x4*)(qg + 4096 + (wq & 1) * 512 + r * 16);
  s16x4 qv[2][2];
#pragma unroll
  for (int a = 0; a < 2; ++a)
#pragma unroll
    for (int j = 0; j < 2; ++j) {
      u32x2 t = {hh == j ? qbb[2 * a] : 0u, hh == j ? qbb[2 * a + 1] : 0u};
      qv[a][j] = __builtin_bit_cast(s16x4, t);
    }
  float kn2;
  {
    const float* kp = (const float*)(ws + WS_KP) + (head * 32 + r) * 8;
    f32x4 a = *(const f32x4*)kp;
    float b = kp[4];
#pragma unroll
    for (int o = 1; o < 32; o <<= 1) {
      a[0] = fmaxf(a[0], __shfl_xor(a[0], o)); a[1] = fmaxf(a[1], __shfl_xor(a[1], o));
      a[2] = fmaxf(a[2], __shfl_xor(a[2], o)); a[3] = fmaxf(a[3], __shfl_xor(a[3], o));
      b = fmaxf(b, __shfl_xor(b, o));
    }
    kn2 = (a[0] + a[1]) + (a[2] + a[3]) + b;
  }
  float mrow[2];
#pragma unroll
  for (int a = 0; a < 2; ++a) {
    const u32x4 u = __builtin_bit_cast(u32x4, qa[a]);
    const u32x2 ub = __builtin_bit_cast(u32x2, qv[a][0]);
    float ss = 0.f;
#pragma unroll
    for (int e = 0; e < 4; ++e) ss += bflo(u[e]) * bflo(u[e]) + bfhi(u[e]) * bfhi(u[e]);
#pragma unroll
    for (int e = 0; e < 2; ++e) ss += bflo(ub[e]) * bflo(ub[e]) + bfhi(ub[e]) * bfhi(ub[e]);
    ss += __shfl_xor(ss, 32);
    mrow[a] = sqrtf(ss * kn2) * 1.02f;
  }
  const int dsel = r <= 20 ? r : (r < 28 ? 20 : 4);
  const int koff = kh * (int)KVGS + lane * 16, voff = kh * (int)KVGS + 5120 + hh * 336 + dsel * 16;

  for (int pass = 0;; ++pass) {
    f32x16 negm[2], sc[2], oacc[2];
#pragma unroll
    for (int a = 0; a < 2; ++a)
#pragma unroll
      for (int e = 0; e < 16; ++e) { negm[a][e] = -mrow[a]; oacc[a][e] = 0.f; }
    asm volatile("" : "+v"(negm[0]), "+v"(negm[1]));
    u32x4 pw[2][2] = {};
    bf16x8 ka, kb, vf[2] = {};
    int sl_cur = 0, sl_next = K2_SLOT, sl_free = 2 * K2_SLOT;
    wait_vm_bar<0>();
    ka = lds128(lds + koff);
    kb = lds128(lds + koff + 4096);
#define KBH(t) (((t) & 1) ? __builtin_shufflevector(kb, kb, 4, 5, 6, 7) : __builtin_shufflevector(kb, kb, 0, 1, 2, 3))
    sc[0] = MFMA32(ka, qa[0], negm[0]);
    sc[0] = MFMA32K8(KBH(0), qv[0][0], sc[0]);
#define EX4(X, i) do { X[i] = __builtin_amdgcn_exp2f(X[i]); X[i + 1] = __builtin_amdgcn_exp2f(X[i + 1]); X[i + 2] = __builtin_amdgcn_exp2f(X[i + 2]); X[i + 3] = __builtin_amdgcn_exp2f(X[i + 3]); } while (0)
#define PK2(P, j, X, i) do { P[j] = cvtpk(X[i], X[i + 1]); P[j + 1] = cvtpk(X[i + 2], X[i + 3]); } while (0)
#define PFR(a, s) __builtin_bit_cast(bf16x8, pw[a][s])
#define PIN(x) asm volatile("" : "+v"(x))
#define UNIT(A, B, TB, LDK, LDV)                                                             \
  do {                                                                                       \
    sc[B] = MFMA32(ka, qa[B], negm[B]); SBAR();                                              \
    EX4(sc[A], 0); PIN(sc[A]); SBAR();                                                       \
    sc[B] = MFMA32K8(KBH(TB), qv[B][(TB) >> 1], sc[B]); SBAR();                              \
    LDK; SBAR();                                                                             \
    EX4(sc[A], 4); PIN(sc[A]); SBAR();                                                       \
    oacc[B] = MFMA32(vf[0], PFR(B, 0), oacc[B]); SBAR();                                     \
    PK2(pw[A][0], 0, sc[A], 0); PK2(pw[A][0], 2, sc[A], 4); EX4(sc[A], 8); PIN(sc[A]); PIN(pw[A][0]); SBAR(); \
    oacc[B] = MFMA32(vf[1], PFR(B, 1), oacc[B]); SBAR();                                     \
    LDV; SBAR();                                                                             \
    EX4(sc[A], 12); PK2(pw[A][1], 0, sc[A], 8); PK2(pw[A][1], 2, sc[A], 12); PIN(pw[A][1]); SBAR(); \
  } while (0)
#define LDV_(t) do { vf[0] = lds128(vp + (t) * 1344); vf[1] = lds128(vp + (t) * 1344 + 672); } while (0)
    for (int c = 0; c < 8; ++c) {
      lds_ptr kp = lds + koff + sl_cur, vp = lds + voff + sl_cur, kn = lds + koff + sl_next;
      UNIT(0, 1, 0, ka = lds128(kp + 1024), LDV_(0));
      UNIT(1, 0, 1, (void)0, (void)0);
      UNIT(0, 1, 1, ka = lds128(kp + 2048), LDV_(1));
      UNIT(1, 0, 2, (void)0, (void)0);
      UNIT(0, 1, 2, ka = lds128(kp + 3072), LDV_(2));
      UNIT(1, 0, 3, (void)0, (void)0);
      wait_vm_bar<0>();
      if (c + 2 < 8) K2_ISSUE(c + 2, sl_free);
      SBAR();
      UNIT(0, 1, 3, do { ka = lds128(kn); kb = lds128(kn + 4096); } while (0), LDV_(3));
      UNIT(1, 0, 0, (void)0, (void)0);
      const int t_ = sl_cur; sl_cur = sl_next; sl_next = sl_free; sl_free = t_;
    }
    oacc[1] = MFMA32(vf[0], PFR(1, 0), oacc[1]);
    oacc[1] = MFMA32(vf[1], PFR(1, 1), oacc[1]);
    wait_lgkm_bar();
    LAS f32x4* ex = (LAS f32x4*)(lds + wid * 3072) + lane;
    {
      const f32x16 o = kh ? oacc[0] : oacc[1];
      ex[0] = f32x4{o[0], o[1], o[2], o[3]}; ex[64] = f32x4{o[4], o[5], o[6], o[7]}; ex[128] = f32x4{o[8], o[9], o[10], o[11]};
    }
    if (threadIdx.x == 0) *(LAS unsigned*)(lds + K2_FLAG) = 0u;
    wait_lgkm_bar();
    const LAS f32x4* in = (const LAS f32x4*)(lds + (wid ^ 4) * 3072) + lane;
    const f32x16 o = kh ? oacc[1] : oacc[0];
    const f32x4 o0 = in[0] + f32x4{o[0], o[1], o[2], o[3]}, o1 = in[64] + f32x4{o[4], o[5], o[6], o[7]}, o2 = in[128] + f32x4{o[8], o[9], o[10], o[11]};
    const float lsum = __shfl(o2[0], r + 32);
    const bool bad = !(lsum >= 0x1p-40f && lsum <= 0x1p40f);
    if (pass == 0) {
      if (__any(bad) && lane == 0) *(LAS unsigned*)(lds + K2_FLAG) = 1u;
      wait_lgkm_bar();
      const unsigned fl = rfl(*(const LAS unsigned*)(lds + K2_FLAG));
      if (fl) {
        wait_lgkm_bar();
        K2_ISSUE(0, 0);
        K2_ISSUE(1, K2_SLOT);
        float mx[2] = {-INFINITY, -INFINITY};
        for (int gi = 0; gi < 16; ++gi) {
          const char* gp = kvg + (size_t)gi * KVGS + lane * 16;
          kb = *(const bf16x8*)(gp + 4096);
#pragma unroll
          for (int t = 0; t < 4; ++t) {
            ka = *(const bf16x8*)(gp + t * 1024);
#pragma unroll
            for (int a = 0; a < 2; ++a) {
              f32x16 s = MFMA32(ka, qa[a], f32x16{});
              s = MFMA32K8(KBH(t), qv[a][t >> 1], s);
#pragma unroll
              for (int e = 0; e < 16; ++e) mx[a] = fmaxf(mx[a], s[e]);
            }
          }
        }
#pragma unroll
        for (int a = 0; a < 2; ++a) mrow[a] = fmaxf(mx[a], __shfl_xor(mx[a], 32));
        continue;
      }
    }
    const float rl = 1.0f / lsum;
    const int tok = qb * 256 + wq * 64 + kh * 32 + r;
    char* op = ws + WS_O + ((size_t)(tok >> 4) * 80 * 16 + (tok & 15)) * 16;
    const int k0 = head * 20 + 4 * hh;
    { u32x2 w = {cvtpk(o0[0] * rl, o0[1] * rl), cvtpk(o0[2] * rl, o0[3] * rl)}; *(u32x2*)(op + (k0 >> 3) * 256 + ((k0 >> 2) & 1) * 8) = w; }
    { u32x2 w = {cvtpk(o1[0] * rl, o1[1] * rl), cvtpk(o1[2] * rl, o1[3] * rl)}; *(u32x2*)(op + ((k0 + 8) >> 3) * 256 + ((k0 >> 2) & 1) * 8) = w; }
    if (hh == 0) { u32x2 w = {cvtpk(o2[0] * rl, o2[1] * rl), cvtpk(o2[2] * rl, o2[3] * rl)}; *(u32x2*)(op + ((k0 + 16) >> 3) * 256 + ((k0 >> 2) & 1) * 8) = w; }
    break;
  }
#undef K2_ISSUE
}

constexpr int K3_STG = 36864, K3_NS = 4, K3_LDS = K3_NS * K3_STG;

__global__ __launch_bounds__(512, 2) void k3_out(const char* __restrict__ x, const char* __restrict__ wo, const char* __restrict__ wd,
                                                 char* __restrict__ ws, char* __restrict__ out) {
  __shared__ __attribute__((aligned(16))) char smem[K3_LDS];
  lds_ptr lds = (lds_ptr)smem;
  const int lane = threadIdx.x & 63, wid = rfl(threadIdx.x >> 6);
  const int bid = blockIdx.x, xcd = bid & 7, idx = bid >> 3;
  const int mb = (xcd >> 1) * 8 + (idx & 7), nb = (xcd & 1) * 4 + (idx >> 3);
  const int r = lane & 15, g = lane >> 4;
  f32x4 acc[5][4];
  if (wid < 4) {
    lds_ptr xa = lds + wid * 9216 + lane * 16;
    lds_ptr wa0 = lds + wid * 9216 + 4096 + (8 * g + ((r >> 2) ^ (4 * (g & 1)))) * 160 + (r & 3) * 8;
    lds_ptr wa1 = lds + wid * 9216 + 4096 + (8 * g + ((4 + (r >> 2)) ^ (4 * (g & 1)))) * 160 + (r & 3) * 8;
#pragma unroll
    for (int i = 0; i < 5; ++i)
#pragma unroll
      for (int j = 0; j < 4; ++j) acc[i][j] = f32x4{0.f, 0.f, 0.f, 0.f};
    bf16x8 xf[2][4], wf[2][5];
#define K3_READ(B, OFF)                                                                                \
  do {                                                                                                 \
    _Pragma("unroll") for (int mt = 0; mt < 4; ++mt) xf[B][mt] = lds128(xa + (OFF) + mt * 1024);       \
    _Pragma("unroll") for (int nt = 0; nt < 5; ++nt) wf[B][nt] = cat8(ldstr(wa0 + (OFF) + nt * 32), ldstr(wa1 + (OFF) + nt * 32)); \
  } while (0)
#define K3_MMA(B)                                                                                      \
  do {                                                                                                 \
    _Pragma("unroll") for (int nt = 0; nt < 5; ++nt)                                                   \
      _Pragma("unroll") for (int mt = 0; mt < 4; ++mt) acc[nt][mt] = MFMA16(wf[B][nt], xf[B][mt], acc[nt][mt]); \
  } while (0)
    wait_lgkm_bar();
    K3_READ(0, 0);
    int off = K3_STG;
    for (int st = 0; st < 16; st += 2) {
      wait_lgkm_bar();
      K3_READ(1, off);
      off = off == (K3_NS - 1) * K3_STG ? 0 : off + K3_STG;
      SBAR();
      K3_MMA(0);
      SBAR();
      wait_lgkm_bar();
      K3_READ(0, off);
      off = off == (K3_NS - 1) * K3_STG ? 0 : off + K3_STG;
      SBAR();
      K3_MMA(1);
      SBAR();
    }
    K3_MMA(0);
#undef K3_READ
#undef K3_MMA
  } else {
    const int lw = wid - 4;
    unsigned vw[5];
#pragma unroll
    for (int j = 0; j < 5; ++j) {
      const int u = j * 64 + lane, pr = u / 10, pos = u - pr * 10, k = pr ^ (((pr >> 3) & 1) << 2);
      vw[j] = k * 1280 + pos * 16 - 1024 * (j & 3);
    }
    unsigned vh[4], vo[4];
#pragma unroll
    for (int i = 0; i < 4; ++i) { vh[i] = lane * 16 + i * (192 * 256 - 1024); vo[i] = lane * 16 + i * (80 * 256 - 1024); }
    const char* hb = ws + WS_H + (size_t)mb * 4 * 192 * 256 + lw * 1024;
    const char* ob = ws + WS_O + (size_t)mb * 4 * 80 * 256 + lw * 1024;
    const char* wdb = wd + nb * 160 + (size_t)lw * 32 * 1280;
    const char* wob = wo + nb * 160 + (size_t)lw * 32 * 1280;
    const unsigned l0 = (unsigned)(size_t)lds + lw * 9216;
#define K3_ISSUE(ST)                                                                                   \
  do {                                                                                                 \
    const unsigned d_ = l0 + ((ST) % K3_NS) * K3_STG;                                                  \
    const char* w_ = (ST) < 12 ? wdb + (size_t)(ST) * (128 * 1280) : wob + (size_t)((ST) - 12) * (128 * 1280); \
    if ((ST) < 12) glds4(vh[0], vh[1], vh[2], vh[3], hb + (ST) * 4096, d_);                            \
    else glds4(vo[0], vo[1], vo[2], vo[3], ob + ((ST) - 12) * 4096, d_);                               \
    glds4(vw[0], vw[1], vw[2], vw[3], w_, d_ + 4096);                                                  \
    glds1(vw[4], w_, d_ + 8192);                                                                       \
  } while (0)
    K3_ISSUE(0); K3_ISSUE(1); K3_ISSUE(2); K3_ISSUE(3);
    wait_vm_bar<27>();
#pragma unroll
    for (int k = 1; k <= 13; ++k) { wait_vm_bar<18>(); K3_ISSUE(k + 3); }
    wait_vm_bar<18>();
    wait_vm_bar<9>();
    wait_vm_bar<0>();
#undef K3_ISSUE
  }
  wait_lgkm_bar();
  if (wid < 4) {
    LAS f32x4* ex = (LAS f32x4*)(lds + wid * 20480) + lane;
#pragma unroll
    for (int nt = 0; nt < 5; ++nt)
#pragma unroll
      for (int mt = 0; mt < 4; ++mt) ex[(nt * 4 + mt) * 64] = acc[nt][mt];
  }
  wait_lgkm_bar();
  for (int u = threadIdx.x; u < 640; u += 512) {
    const int tok = u / 10, c = u - tok * 10;
    const int fr = (c >> 1) * 4 + (tok >> 4), ln = ((c & 1) * 2) * 16 + (tok & 15);
    const LAS f32x4* in = (const LAS f32x4*)lds + fr * 64 + ln;
    f32x4 a = in[0], b = in[16];
#pragma unroll
    for (int s = 1; s < 4; ++s) { a += in[s * 1280]; b += in[s * 1280 + 16]; }
    const size_t go = ((size_t)(mb * 64 + tok) * 640 + nb * 80 + c * 8) * 2;
    const u32x4 xv = *(const u32x4*)(x + go);
    u32x4 o = {cvtpk(bflo(xv[0]) + a[0], bfhi(xv[0]) + a[1]), cvtpk(bflo(xv[1]) + a[2], bfhi(xv[1]) + a[3]),
               cvtpk(bflo(xv[2]) + b[0], bfhi(xv[2]) + b[1]), cvtpk(bflo(xv[3]) + b[2], bfhi(xv[3]) + b[3])};
    *(u32x4*)(out + go) = o;
  }
}

extern "C" void kernel_launch(void* const* d_in, const int* in_sizes, int n_in, void* d_out, int out_size, void* d_ws, size_t ws_size,
                              hipStream_t stream) {
  const char* x = (const char*)d_in[0];
  const char* wqkv = (const char*)d_in[1];
  const char* wo = (const char*)d_in[2];
  const char* wg = (const char*)d_in[4];
  const char* wu = (const char*)d_in[5];
  const char* wd = (const char*)d_in[6];
  char* ws = (char*)d_ws;
  k1_qkv<<<240, 512, 0, stream>>>(x, wqkv, ws);
  kg_gateup<<<256, 512, 0, stream>>>(wg, wu, ws);
  k2_attn<<<256, 512, 0, stream>>>(ws);
  k3_out<<<256, 512, 0, stream>>>(x, wo, wd, ws, (char*)d_out);
}
```

```cpp
#include <hip/hip_runtime.h>
#include <hip/hip_bf16.h>
#include <stdint.h>

#define DI __device__ __forceinline__
#define LAS __attribute__((address_space(3)))
#define GAS __attribute__((address_space(1)))
typedef short bf16x8 __attribute__((ext_vector_type(8)));
typedef short s16x4 __attribute__((ext_vector_type(4)));
typedef float f32x4 __attribute__((ext_vector_type(4)));
typedef float f32x2 __attribute__((ext_vector_type(2)));
typedef float f32x16 __attribute__((ext_vector_type(16)));
typedef unsigned u32x4 __attribute__((ext_vector_type(4)));
typedef unsigned u32x2 __attribute__((ext_vector_type(2)));
typedef __bf16 bf16x2_t __attribute__((ext_vector_type(2)));
typedef LAS char* lds_ptr;
typedef unsigned long long u64;
typedef GAS u64 gu64;

constexpr int S = 2048, D = 640, F = 1536;
constexpr size_t QGS = 5120, QHS = 16 * QGS;
constexpr size_t KVGS = 10496, KVHS = 16 * KVGS;
constexpr size_t WS_EP = 0;
constexpr size_t WS_FQ = 4096;
constexpr size_t WS_FH = WS_FQ + 2048;
constexpr size_t WS_FO = WS_FH + 2048;
constexpr size_t WS_FW = WS_FO + 2048;
constexpr size_t WS_ST = 16384;
constexpr size_t WS_CG = WS_ST + 2048 * 8;
constexpr size_t WS_KP = WS_CG + 16 * 3072 * 8;
constexpr size_t WS_CS = WS_KP + 32 * 32 * 8 * 4;
constexpr size_t WS_Q = 1048576;
constexpr size_t WS_KV = WS_Q + 32 * QHS;
constexpr size_t WS_H = WS_KV + 32 * KVHS + 4096;
constexpr size_t WS_O = WS_H + (size_t)S * F * 2;
constexpr size_t WS_WP = WS_O + (size_t)S * D * 2;
static_assert(WS_CS + 15 * 16 * 128 * 8 <= WS_Q, "ws map");

#define SBAR() __builtin_amdgcn_sched_barrier(0)
#define PIN(x) asm volatile("" : "+v"(x))
constexpr size_t WS_DBG = (size_t)128 << 20;
#define TS() do {} while (0)
#define TARGS
#define TPASS
#define WVB(N) wait_vm_bar_<N>()
#define WLB() wait_lgkm_bar_()
#define BAR() bar_()
#define MFMA16(a, b, c) __builtin_amdgcn_mfma_f32_16x16x32_bf16(a, b, c, 0, 0, 0)
#define MFMA32(a, b, c) __builtin_amdgcn_mfma_f32_32x32x16_bf16(a, b, c, 0, 0, 0)
#define MFMA32K8(a, b, c) __builtin_amdgcn_mfma_f32_32x32x8bf16_1k(a, b, c, 0, 0, 0)
#define RLX_AGENT __ATOMIC_RELAXED, __HIP_MEMORY_SCOPE_AGENT
#define NOPK 0
#define DOSE 0
constexpr unsigned kSpinCap = 1u << 17;
constexpr int LDS_BYTES = 163840;

DI unsigned f2u(float x) { return __builtin_bit_cast(unsigned, x); }
DI float u2f(unsigned x) { return __builtin_bit_cast(float, x); }
DI bool wall(bool p) { return __builtin_amdgcn_ballot_w64(!p) == 0; }
DI bool wany(bool p) { return __builtin_amdgcn_ballot_w64(p) != 0; }
DI float adds(float a, float b) { float r; asm("v_add_f32 %0, %1, %2" : "=v"(r) : "v"(a), "v"(b)); return r; }
DI float fmas(float a, float b, float c) { float r; asm("v_fma_f32 %0, %1, %2, %3" : "=v"(r) : "v"(a), "v"(b), "v"(c)); return r; }
DI unsigned rfl(unsigned x) { return __builtin_amdgcn_readfirstlane(x); }
DI unsigned cvtpk(float lo, float hi) { f32x2 v = {lo, hi}; bf16x2_t b = __builtin_convertvector(v, bf16x2_t); return __builtin_bit_cast(unsigned, b); }
DI float bfr(float x) { return u2f(cvtpk(x, 0.f) << 16); }
DI float bflo(unsigned u) { return u2f(u << 16); }
DI float bfhi(unsigned u) { return u2f(u & 0xffff0000u); }
DI bf16x8 lds128(lds_ptr p) { return *(const LAS bf16x8*)p; }
DI s16x4 lds64(lds_ptr p) { return *(const LAS s16x4*)p; }
DI s16x4 ldstr(lds_ptr p) { return __builtin_amdgcn_ds_read_tr16_b64_v4i16((LAS s16x4*)p); }
DI bf16x8 cat8(s16x4 a, s16x4 b) { return __builtin_shufflevector(a, b, 0, 1, 2, 3, 4, 5, 6, 7); }
template <int N> DI void wait_vm_bar_() { asm volatile("s_waitcnt vmcnt(%0) lgkmcnt(0)\n\ts_barrier" ::"i"(N) : "memory"); }
DI void wait_lgkm_bar_() { asm volatile("s_waitcnt lgkmcnt(0)\n\ts_barrier" ::: "memory"); }
DI void st16_sc1(char* p, u32x4 v) { asm volatile("global_store_dwordx4 %0, %1, off sc1\n\ts_nop 1" ::"v"(p), "v"(v) : "memory"); }
DI void st16_sc1(const char* sb, unsigned off, u32x4 v) { asm volatile("global_store_dwordx4 %0, %1, %2 sc1\n\ts_nop 1" ::"v"(off), "v"(v), "s"(sb) : "memory"); }
DI void st8_sc1(char* p, u32x2 v) { asm volatile("global_store_dwordx2 %0, %1, off sc1\n\ts_nop 1" ::"v"(p), "v"(v) : "memory"); }
DI u32x4 ld16_sc1_wait(const char* p) { u32x4 v; asm volatile("global_load_dwordx4 %0, %1, off sc1\n\ts_waitcnt vmcnt(0)" : "=&v"(v) : "v"(p) : "memory"); return v; }

DI void glds2(unsigned v0, unsigned v1, const void* sbase, unsigned m0v) {
  unsigned sv;
  asm volatile("s_mov_b32 %0, m0\n\ts_mov_b32 m0, %4\n\ts_nop 0\n\t"
               "global_load_lds_dwordx4 %1, %3\n\t"
               "global_load_lds_dwordx4 %2, %3 offset:1024\n\t"
               "s_mov_b32 m0, %0"
               : "=&s"(sv) : "v"(v0), "v"(v1), "s"(sbase), "s"(m0v) : "memory");
}
DI void glds5(const unsigned (&v)[5], const void* sbase, unsigned m0v) {
  unsigned sv;
  asm volatile("s_waitcnt lgkmcnt(0)\n\ts_mov_b32 %0, m0\n\ts_mov_b32 m0, %7\n\ts_nop 0\n\t"
               "global_load_lds_dwordx4 %1, %6\n\t"
               "global_load_lds_dwordx4 %2, %6 offset:1024\n\t"
               "global_load_lds_dwordx4 %3, %6 offset:2048\n\t"
               "global_load_lds_dwordx4 %4, %6 offset:3072\n\t"
               "s_add_u32 m0, m0, 4096\n\ts_nop 0\n\t"
               "global_load_lds_dwordx4 %5, %6\n\t"
               "s_mov_b32 m0, %0"
               : "=&s"(sv) : "v"(v[0]), "v"(v[1]), "v"(v[2]), "v"(v[3]), "v"(v[4]), "s"(sbase), "s"(m0v) : "memory", "scc");
}
DI void wait_tag(const char* p0, const char* p1, u64 tag) {
  _Pragma("unroll 1") for (unsigned spins = 0; spins < kSpinCap; ++spins) {
    const u64 a = __hip_atomic_load((gu64*)p0, RLX_AGENT), b = __hip_atomic_load((gu64*)p1, RLX_AGENT);
    if (wall(a == tag && b == tag)) break;
    __builtin_amdgcn_s_sleep(2);
  }
  __builtin_amdgcn_fence(__ATOMIC_ACQUIRE, "wavefront");
}
DI void wait_tag(const char* p0, u64 tag) {
  _Pragma("unroll 1") for (unsigned spins = 0; spins < kSpinCap; ++spins) {
    const u64 a = __hip_atomic_load((gu64*)p0, RLX_AGENT);
    if (wall(a == tag)) break;
    __builtin_amdgcn_s_sleep(2);
  }
  __builtin_amdgcn_fence(__ATOMIC_ACQUIRE, "wavefront");
}
DI void set_flag(char* p, u64 tag) { __hip_atomic_store((gu64*)p, tag, RLX_AGENT); }
DI u64 make_tag(u64 e) {
  const u64 u = ((u64)rfl((unsigned)(e >> 32)) << 32) | rfl((unsigned)e);
  return u * 0x9E3779B97F4A7C15ull + 0xD1B54A32D192ED03ull;
}

DI void glds4(unsigned v0, unsigned v1, unsigned v2, unsigned v3, const void* sbase, unsigned m0v) {
  unsigned sv;
  asm volatile("s_mov_b32 %0, m0\n\ts_mov_b32 m0, %6\n\ts_nop 0\n\t"
               "global_load_lds_dwordx4 %1, %5\n\t"
               "global_load_lds_dwordx4 %2, %5 offset:1024\n\t"
               "global_load_lds_dwordx4 %3, %5 offset:2048\n\t"
               "global_load_lds_dwordx4 %4, %5 offset:3072\n\t"
               "s_mov_b32 m0, %0"
               : "=&s"(sv) : "v"(v0), "v"(v1), "v"(v2), "v"(v3), "s"(sbase), "s"(m0v) : "memory");
}
DI void glds4_sc1(unsigned v0, unsigned v1, unsigned v2, unsigned v3, const void* sbase, unsigned m0v) {
  unsigned sv;
  asm volatile("s_mov_b32 %0, m0\n\ts_mov_b32 m0, %6\n\ts_nop 0\n\t"
               "global_load_lds_dwordx4 %1, %5 sc1\n\t"
               "global_load_lds_dwordx4 %2, %5 offset:1024 sc1\n\t"
               "global_load_lds_dwordx4 %3, %5 offset:2048 sc1\n\t"
               "global_load_lds_dwordx4 %4, %5 offset:3072 sc1\n\t"
               "s_mov_b32 m0, %0"
               : "=&s"(sv) : "v"(v0), "v"(v1), "v"(v2), "v"(v3), "s"(sbase), "s"(m0v) : "memory");
}
DI void glds1_sc1(unsigned v0, const void* sbase, unsigned m0v) {
  unsigned sv;
  asm volatile("s_mov_b32 %0, m0\n\ts_mov_b32 m0, %3\n\ts_nop 0\n\tglobal_load_lds_dwordx4 %1, %2 sc1\n\ts_mov_b32 m0, %0"
               : "=&s"(sv) : "v"(v0), "s"(sbase), "s"(m0v) : "memory");
}
DI void touch(unsigned voff, const void* sbase, unsigned m0v) {
  unsigned sv;
  asm volatile("s_mov_b32 %0, m0\n\ts_mov_b32 m0, %3\n\ts_nop 0\n\tglobal_load_lds_dword %1, %2\n\ts_mov_b32 m0, %0"
               : "=&s"(sv) : "v"(voff), "s"(sbase), "s"(m0v) : "memory");
}
DI void touch_sc1(unsigned voff, const void* sbase, unsigned m0v) {
  unsigned sv;
  asm volatile("s_mov_b32 %0, m0\n\ts_mov_b32 m0, %3\n\ts_nop 0\n\tglobal_load_lds_dword %1, %2 sc1\n\ts_mov_b32 m0, %0"
               : "=&s"(sv) : "v"(voff), "s"(sbase), "s"(m0v) : "memory");
}
DI void st_gran(char* p, unsigned val, u64 tag) { __hip_atomic_store((gu64*)p, ((u64)(unsigned)tag << 32) | val, RLX_AGENT); }
#define SGB(mask, n) __builtin_amdgcn_sched_group_barrier(mask, n, 0)

constexpr int WR = 65536;
constexpr int GW0 = WR + 57344;
constexpr int SCR = 147456;
constexpr int GWS = WR + 65536;
constexpr int SCR_Q = SCR;
constexpr int SCR_STAT = 159744, SCR_CS = SCR_STAT + 1024, SCR_CG = SCR_STAT + 1536, SCR_TAG = SCR_STAT + 2304, SCR_KN = SCR_STAT + 2320, SCR_M = SCR_STAT + 2560;
constexpr int SCR_DUMMY = 163584;
constexpr int AT_CH = 2 * (int)KVGS;
constexpr int AT_A = 0, AT_B = 98304, AT_C = 32768, AT_H = 121856;
constexpr int AT_RP = 57344;
constexpr int AT_FLAG = 110592, AT_EX = AT_FLAG + 1024;
constexpr int D_STG = 36864;

template <int CTRL> DI float dpp(float x) { return u2f(__builtin_amdgcn_update_dpp(0u, f2u(x), CTRL, 0xf, 0xf, true)); }
DI float sum8(float x) { x += dpp<0xB1>(x); x += dpp<0x4E>(x); x += dpp<0x141>(x); return x; }
DI float sum16(float x) { x = sum8(x); x += dpp<0x140>(x); return x; }
DI float max16(float x) { x = fmaxf(x, dpp<0xB1>(x)); x = fmaxf(x, dpp<0x4E>(x)); x = fmaxf(x, dpp<0x141>(x)); x = fmaxf(x, dpp<0x140>(x)); return x; }
DI float sumx16(float x) { auto r = __builtin_amdgcn_permlane16_swap(f2u(x), f2u(x), false, false); return u2f(r[0]) + u2f(r[1]); }
DI float sumx32(float x) { auto r = __builtin_amdgcn_permlane32_swap(f2u(x), f2u(x), false, false); return adds(u2f(r[0]), u2f(r[1])); }
DI float maxx16(float x) { auto r = __builtin_amdgcn_permlane16_swap(f2u(x), f2u(x), false, false); return fmaxf(u2f(r[0]), u2f(r[1])); }
DI float maxx32(float x) { auto r = __builtin_amdgcn_permlane32_swap(f2u(x), f2u(x), false, false); return fmaxf(u2f(r[0]), u2f(r[1])); }
DI u32x4 bld16_sc1(__amdgpu_buffer_rsrc_t rs, unsigned off) { return __builtin_amdgcn_raw_buffer_load_b128(rs, off, 0, 16); }
DI u32x2 bld8_sc1(__amdgpu_buffer_rsrc_t rs, unsigned off) { return __builtin_amdgcn_raw_buffer_load_b64(rs, off, 0, 16); }
DI float sq2(unsigned u, float c) { const bf16x2_t v = __builtin_bit_cast(bf16x2_t, u); return __builtin_amdgcn_fdot2_f32_bf16(v, v, c, false); }
DI void bar_() { asm volatile("s_barrier" ::: "memory"); }

template <bool SWAP>
DI void qkv_compute(lds_ptr lds, int cw, int lane, int rb, int cb, char* ws TARGS) {
  const int wm = cw >> 1, wn = cw & 1, r = lane & 15, g = lane >> 4;
  lds_ptr xa[2], wa[4];
#pragma unroll
  for (int ks = 0; ks < 2; ++ks) xa[ks] = lds + (wm * 64 + r) * 128 + (((4 * ks + g) ^ ((r >> 1) & 7)) * 16);
  {
    const int q = r >> 2, p = r & 3, f = q | ((g & 1) << 2);
#pragma unroll
    for (int nt = 0; nt < 4; ++nt) wa[nt] = lds + WR + (8 * g + q) * 256 + (((wn * 4 + nt) ^ f) * 32) + p * 8;
  }
  f32x4 acc[4][4];
#pragma unroll
  for (int i = 0; i < 4; ++i)
#pragma unroll
    for (int j = 0; j < 4; ++j) acc[i][j] = f32x4{0.f, 0.f, 0.f, 0.f};
  bf16x8 xf[2][4], wf[2][4];
#define Q_READ(B, SO, KS)                                                                                           \
  do {                                                                                                              \
    _Pragma("unroll") for (int i = 0; i < 4; ++i) {                                                                 \
      wf[B][i] = cat8(ldstr(wa[i] + (SO) + (KS) * 8192), ldstr(wa[i] + (SO) + (KS) * 8192 + 1024));                 \
      xf[B][i] = lds128(xa[KS] + (SO) + i * 2048);                                                                  \
    }                                                                                                               \
  } while (0)
#define Q_MMA(B)                                                                                                    \
  do {                                                                                                              \
    _Pragma("unroll") for (int nt = 0; nt < 4; ++nt)                                                                \
      _Pragma("unroll") for (int mt = 0; mt < 4; ++mt)                                                              \
        acc[nt][mt] = SWAP ? MFMA16(wf[B][nt], xf[B][mt], acc[nt][mt]) : MFMA16(xf[B][mt], wf[B][nt], acc[nt][mt]); \
  } while (0)
#define Q_MIX() do { _Pragma("unroll") for (int i = 0; i < 12; ++i) { SGB(0x8, 1); SGB(0x100, 1); } SGB(0x8, 4); } while (0)
  WVB(1);
  Q_READ(0, 0, 0);
  int so = 0;
#pragma unroll 1
  for (int st = 0; st < 9; ++st) {
    SBAR();
    Q_READ(1, so, 1); Q_MMA(0); Q_MIX();
    SBAR();
    WLB();
    so = (so + 16384) & 65535;
    SBAR();
    Q_READ(0, so, 0); Q_MMA(1); Q_MIX();
  }
  SBAR();
  Q_READ(1, 16384, 1); Q_MMA(0); Q_MIX();
  SBAR();
  Q_MMA(1);
#undef Q_READ
#undef Q_MMA
#undef Q_MIX
  const LAS f32x2* stat = (const LAS f32x2*)(lds + SCR_STAT);
  const LAS float* csum = (const LAS float*)(lds + SCR_CS);
  const int kind = cb / 5, fb = (cb - kind * 5) * 128 + wn * 64;
  if (SWAP) {
    f32x2 st4[4];
    const float qs = kind == 0 ? 0.22360679774997896f * 1.4426950408889634f : 1.0f;
#pragma unroll
    for (int mt = 0; mt < 4; ++mt) { st4[mt] = stat[wm * 64 + mt * 16 + r]; st4[mt][1] *= qs; }
    const char* img = kind == 0 ? ws + WS_Q + (size_t)rb * QGS : ws + WS_KV + (size_t)rb * KVGS;
    const unsigned hs = kind == 0 ? (unsigned)QHS : (unsigned)KVHS;
    const unsigned rowoff = kind == 0 ? r * 16 : ((r >> 3) * 16 + (r & 7)) * 16, mstr = kind == 0 ? 256 : 128;
    float nmax[4]; unsigned kpo[4];
#pragma unroll
    for (int nt = 0; nt < 4; ++nt) {
      const int nl = nt * 16 + 4 * g;
      const f32x4 cs = *(const LAS f32x4*)(csum + wn * 64 + nl);
      const int f0 = fb + nl, head = f0 / 20, sl = (f0 - head * 20) >> 2;
      const unsigned base = head * hs + sl * 1024 + wm * 512 + rowoff;
      nmax[nt] = 0.f; kpo[nt] = (head * 32 + rb * 2 + wm) * 8 + sl;
      unsigned pk[4][2];
#pragma unroll
      for (int mt = 0; mt < 4; ++mt) {
        float v[4];
#pragma unroll
        for (int e = 0; e < 4; ++e) v[e] = (acc[nt][mt][e] - st4[mt][0] * cs[e]) * st4[mt][1];
        pk[mt][0] = cvtpk(v[0], v[1]); pk[mt][1] = cvtpk(v[2], v[3]);
        nmax[nt] = fmaxf(nmax[nt], sq2(pk[mt][1], sq2(pk[mt][0], 0.f)));
      }
      st16_sc1(img, base, u32x4{pk[0][0], pk[0][1], pk[2][0], pk[2][1]});
      st16_sc1(img, base + mstr, u32x4{pk[1][0], pk[1][1], pk[3][0], pk[3][1]});
    }
    if (kind == 1) {
      const bool h0 = r & 1, h1 = r & 2;
#pragma unroll
      for (int nt = 0; nt < 4; ++nt) nmax[nt] = max16(nmax[nt]);
      const float a = h1 ? (h0 ? nmax[3] : nmax[2]) : (h0 ? nmax[1] : nmax[0]);
      const unsigned o = h1 ? (h0 ? kpo[3] : kpo[2]) : (h0 ? kpo[1] : kpo[0]);
      if (r < 4) __hip_atomic_store((GAS float*)(ws + WS_KP) + o, a, RLX_AGENT);
    }
  } else {
    const char* img = ws + WS_KV + (size_t)rb * KVGS + 5120;
    unsigned off[4]; float cs[4];
#pragma unroll
    for (int nt = 0; nt < 4; ++nt) {
      const int nl = nt * 16 + r, f = fb + nl, head = f / 20, d = f - head * 20;
      cs[nt] = csum[wn * 64 + nl];
      off[nt] = head * (unsigned)KVHS + d * 16 + (wm * 4 + (g >> 1)) * 672 + (g & 1) * 336;
    }
#pragma unroll
    for (int j = 0; j < 2; ++j) {
      f32x4 sa[2], sb[2];
#pragma unroll
      for (int i = 0; i < 2; ++i) {
        sa[i] = *(const LAS f32x4*)(stat + wm * 64 + (2 * j + i) * 16 + 4 * g);
        sb[i] = *(const LAS f32x4*)(stat + wm * 64 + (2 * j + i) * 16 + 4 * g + 2);
      }
#pragma unroll
      for (int nt = 0; nt < 4; ++nt) {
        u32x4 o;
#pragma unroll
        for (int i = 0; i < 2; ++i) {
          const f32x4 a = acc[nt][2 * j + i];
          o[2 * i] = cvtpk((a[0] - sa[i][0] * cs[nt]) * sa[i][1], (a[1] - sa[i][2] * cs[nt]) * sa[i][3]);
          o[2 * i + 1] = cvtpk((a[2] - sb[i][0] * cs[nt]) * sb[i][1], (a[3] - sb[i][2] * cs[nt]) * sb[i][3]);
        }
        st16_sc1(img, off[nt] + j * 1344, o);
      }
    }
    {
      const int t = cw * 64 + lane, h0 = ((cb - 10) * 128) / 20, h1 = ((cb - 9) * 128) / 20, hd = h0 + (t >> 4);
      if (hd < h1) st16_sc1(img, hd * (unsigned)KVHS + (t & 15) * 336 + 320, u32x4{0x3f803f80u, 0x3f803f80u, 0x3f803f80u, 0x3f803f80u});
    }
  }
}

DI void gu_compute(lds_ptr lds, int cw, int lane, int rb, int fbk, char* ws TARGS) {
  const int wm = cw >> 1, wn = cw & 1, r = lane & 15, g = lane >> 4;
  lds_ptr xa[2], wa[3];
#pragma unroll
  for (int ks = 0; ks < 2; ++ks) xa[ks] = lds + (wm * 64 + r) * 128 + (((4 * ks + g) ^ ((r >> 1) & 7)) * 16);
#pragma unroll
  for (int nt = 0; nt < 3; ++nt) wa[nt] = lds + (8 * g + (r >> 2)) * 192 + (((wn * 3 + nt) ^ (g & 1)) * 32) + (r & 3) * 8;
  f32x4 acc[6][4];
#pragma unroll
  for (int i = 0; i < 6; ++i)
#pragma unroll
    for (int j = 0; j < 4; ++j) acc[i][j] = f32x4{0.f, 0.f, 0.f, 0.f};
  bf16x8 xf[2][4], wf[2][6];
#define G_READ(B, SO, WO, KS)                                                                                           \
  do {                                                                                                              \
    _Pragma("unroll") for (int i = 0; i < 6; ++i) {                                                                 \
      wf[B][i] = cat8(ldstr(wa[i % 3] + (WO) + (i / 3) * 12288 + (KS) * 6144), ldstr(wa[i % 3] + (WO) + (i / 3) * 12288 + (KS) * 6144 + 768)); \
      if (i < 4) xf[B][i] = lds128(xa[KS] + (SO) + i * 2048);                                                       \
    }                                                                                                               \
  } while (0)
#define G_MMA(B)                                                                                                    \
  do {                                                                                                              \
    _Pragma("unroll") for (int nt = 0; nt < 6; ++nt)                                                                \
      _Pragma("unroll") for (int mt = 0; mt < 4; ++mt) acc[nt][mt] = MFMA16(wf[B][nt], xf[B][mt], acc[nt][mt]);     \
  } while (0)
#define G_MIX() do { _Pragma("unroll") for (int i = 0; i < 16; ++i) { SGB(0x8, 1); SGB(0x100, 1); } SGB(0x8, 8); } while (0)
  int so = 32768, wo = GWS;
  WLB();
  G_READ(0, so, wo, 0);
#pragma unroll 1
  for (int st = 10; st < 19; ++st) {
    SBAR();
    G_READ(1, so, wo, 1); G_MMA(0); G_MIX();
    SBAR();
    if (st == 11) asm volatile("s_waitcnt vmcnt(0)" ::: "memory");
    WLB();
    so = (so + 16384) & 65535;
    wo = wo == GW0 - 49152 ? GW0 : wo == GWS ? GW0 - 24576 : wo - 24576;
    SBAR();
    G_READ(0, so, wo, 0); G_MMA(1); G_MIX();
  }
  SBAR();
  G_READ(1, 49152, GW0, 1); G_MMA(0); G_MIX();
  SBAR();
  WLB();
  G_MMA(1);
#undef G_READ
#undef G_MMA
#undef G_MIX
  const LAS f32x2* stat = (const LAS f32x2*)(lds + SCR_STAT);
  const LAS float* csum = (const LAS float*)(lds + SCR_CG);
  f32x2 st4[4];
#pragma unroll
  for (int mt = 0; mt < 4; ++mt) { st4[mt] = stat[wm * 64 + mt * 16 + r]; st4[mt][1] *= st4[mt][1]; }
  const unsigned ho = (((rb * 8 + wm * 4 + (g & 1)) * 192 + fbk * 12 + wn * 6 + (g >> 1)) * 16 + r) * 16;
#pragma unroll
  for (int nt = 0; nt < 3; ++nt) {
    const f32x4 cg = *(const LAS f32x4*)(csum + wn * 48 + nt * 16 + 4 * g), cu = *(const LAS f32x4*)(csum + 96 + wn * 48 + nt * 16 + 4 * g);
    unsigned w[4][2];
#pragma unroll
    for (int mt = 0; mt < 4; ++mt) {
      float hv[4];
#pragma unroll
      for (int e = 0; e < 4; ++e) {
        const float gv = acc[nt][mt][e] - st4[mt][0] * cg[e], uv = acc[3 + nt][mt][e] - st4[mt][0] * cu[e];
        hv[e] = fmaxf(gv, 0.f) * (uv * st4[mt][1]);
      }
      w[mt][0] = cvtpk(hv[0], hv[1]); w[mt][1] = cvtpk(hv[2], hv[3]);
    }
#pragma unroll
    for (int j = 0; j < 2; ++j) {
      auto s0 = __builtin_amdgcn_permlane16_swap(w[2 * j][0], w[2 * j + 1][0], false, false);
      auto s1 = __builtin_amdgcn_permlane16_swap(w[2 * j][1], w[2 * j + 1][1], false, false);
      st16_sc1(ws + WS_H, ho + j * (2 * 192 * 256) + nt * 512, u32x4{s0[0], s1[0], s0[1], s1[1]});
    }
  }
}

#if defined(__HIP_DEVICE_COMPILE__) && NOPK
#define KATTR __attribute__((target("no-packed-fp32-ops")))
#else
#define KATTR
#endif
__global__ __launch_bounds__(512, 2) KATTR void fused_block(const char* __restrict__ x, const char* __restrict__ wqkv, const char* __restrict__ wo,
                                                      const char* __restrict__ wg, const char* __restrict__ wu, const char* __restrict__ wd,
                                                      char* __restrict__ ws, char* __restrict__ out) {
  __shared__ __attribute__((aligned(16))) char smem[LDS_BYTES];
  lds_ptr lds = (lds_ptr)smem;
  const unsigned ldsb = (unsigned)(size_t)lds;
  const int lane = threadIdx.x & 63, wid = rfl(threadIdx.x >> 6), bid = blockIdx.x;
  const int xcd = bid & 7, idx = bid >> 3;
  const int tq = (xcd & 1) * 30 + idx, cb = tq >> 2, rb = (xcd >> 1) * 4 + (tq & 3);
  const int fbk = (xcd & 1) * 8 + (idx >> 2);
  const int head = xcd * 4 + (idx >> 3), qb = idx & 7;
  const bool has_qkv = idx < 30;
  const int rot_g = ((tq & 3) * 5 + 1) / 2 + (xcd >> 1);
  const int sg0 = rot_g >= 10 ? rot_g - 10 : rot_g, sq0 = (rot_g + cb) % 10;
#define NEXT10(k) ((k) == 9 ? 0 : (k) + 1)
  const int r32 = lane & 31, hh = lane >> 5;
  const char* kvg = ws + WS_KV + (size_t)head * KVHS;
  const unsigned vl = lane * 16;
  u64 tag = 0;
  bf16x8 qa[2]; s16x4 qv[2][2]; float mrow[2];
#define AT_ISSUE(C, SLOT)                                                                                      \
  do {                                                                                                         \
    const char* src_ = kvg + (size_t)(C) * AT_CH;                                                              \
    const unsigned dst_ = ldsb + (SLOT);                                                                       \
    unsigned sv_;                                                                                              \
    asm volatile("s_mov_b32 %0, m0\n\ts_mov_b32 m0, %5\n\ts_nop 0\n\t"                                         \
                 "global_load_lds_dwordx4 %1, %2 sc1\n\tglobal_load_lds_dwordx4 %1, %2 offset:1024 sc1\n\t"    \
                 "global_load_lds_dwordx4 %1, %2 offset:2048 sc1\n\tglobal_load_lds_dwordx4 %1, %2 offset:3072 sc1\n\t" \
                 "s_mov_b32 m0, %6\n\ts_nop 0\n\tglobal_load_lds_dwordx4 %1, %3 sc1\n\t"                       \
                 "s_mov_b32 m0, %7\n\ts_mov_b32 exec_hi, 0\n\tglobal_load_lds_dwordx4 %1, %4 sc1\n\t"          \
                 "s_mov_b32 exec_hi, -1\n\ts_mov_b32 m0, %0"                                                   \
                 : "=&s"(sv_)                                                                                  \
                 : "v"(vl), "s"(src_ + wid * 4096), "s"(src_ + 16384 + wid * 1024), "s"(src_ + 20480), "s"(rfl(dst_ + wid * 4096)), \
                   "s"(rfl(dst_ + 16384 + wid * 1024)), "s"(rfl(dst_ + 20480))                                           \
                 : "memory");                                                                                  \
  } while (0)

#define AT_ISSUE2(C, SLOT)                                                                                     \
  do {                                                                                                         \
    const char* src_ = kvg + (size_t)(C) * AT_CH;                                                              \
    const unsigned dst_ = ldsb + (SLOT);                                                                       \
    glds4_sc1(vl, vl, vl, vl, src_ + wid * 10240, rfl(dst_ + wid * 10240));                                    \
    glds4_sc1(vl, vl, vl, vl, src_ + wid * 10240 + 4096, rfl(dst_ + wid * 10240 + 4096));                      \
    unsigned sv_;                                                                                              \
    asm volatile("s_mov_b32 %0, m0\n\ts_mov_b32 m0, %4\n\ts_nop 0\n\t"                                         \
                 "global_load_lds_dwordx4 %1, %2 sc1\n\tglobal_load_lds_dwordx4 %1, %2 offset:1024 sc1\n\t"    \
                 "s_mov_b32 m0, %5\n\ts_mov_b32 exec_hi, 0\n\tglobal_load_lds_dwordx4 %1, %3 sc1\n\t"          \
                 "s_mov_b32 exec_hi, -1\n\ts_mov_b32 m0, %0"                                                   \
                 : "=&s"(sv_)                                                                                  \
                 : "v"(vl), "s"(src_ + wid * 10240 + 8192), "s"(src_ + 20480), "s"(rfl(dst_ + wid * 10240 + 8192)), "s"(rfl(dst_ + 20480)) \
                 : "memory");                                                                                  \
  } while (0)
  u32x4 rv0, rv1; u32x2 gep;
  asm volatile("global_load_dwordx4 %0, %3, %5\n\tglobal_load_dwordx4 %1, %4, %5 offset:1024\n\tglobal_load_dwordx2 %2, %6, %7 sc1"
               : "=&v"(rv0), "=&v"(rv1), "=&v"(gep) : "v"(vl), "v"(vl & 255), "s"(x + (size_t)(bid * 8 + wid) * 1280), "v"(0u), "s"(ws + WS_EP) : "memory");
  unsigned vx[4], vw[4];
#pragma unroll
  for (int i = 0; i < 4; ++i) {
    const int row = 8 * i + (lane >> 3), ch = (lane & 7) ^ ((4 * i + (lane >> 4)) & 7);
    vx[i] = row * 1280 + ch * 16 - 1024 * i;
    const int k = 4 * i + (lane >> 4), f = (lane >> 4) | ((i >> 1) << 2), cw = (lane & 15) ^ (f << 1);
    vw[i] = k * 3840 + cw * 16 - 1024 * i;
  }
  if (has_qkv) {
    if (wid < 4) {
      const char* p = x + (size_t)(rb * 128 + wid * 32) * 1280;
      glds4(vx[0], vx[1], vx[2], vx[3], p + sq0 * 128, ldsb + wid * 4096);
    } else {
      const char* p = wqkv + cb * 256 + (size_t)(wid - 4) * 16 * 3840;
      glds4(vw[0], vw[1], vw[2], vw[3], p + (size_t)sq0 * (64 * 3840), ldsb + WR + (wid - 4) * 4096);
    }
  }
  {
    if (!has_qkv) asm volatile("s_waitcnt vmcnt(0)" ::: "memory");
    asm volatile("s_waitcnt vmcnt(4)" : "+v"(rv0), "+v"(rv1), "+v"(gep)::"memory");
    tag = make_tag(((u64)gep[1] << 32) | gep[0]);
    float s0 = 0.f, s1 = 0.f;
#pragma unroll
    for (int e = 0; e < 4; ++e) { s0 += bflo(rv0[e]) + bfhi(rv0[e]); s1 += bflo(rv1[e]) + bfhi(rv1[e]); }
    const float mean = sumx32(sumx16(sum16(lane < 16 ? s0 + s1 : s0))) * (1.0f / D);
    float q0 = 0.f, q1 = 0.f;
#pragma unroll
    for (int e = 0; e < 4; ++e) {
      const float a = bflo(rv0[e]) - mean, b = bfhi(rv0[e]) - mean, c = bflo(rv1[e]) - mean, d = bfhi(rv1[e]) - mean;
      q0 += a * a + b * b; q1 += c * c + d * d;
    }
    const float den = sqrtf(bfr(bfr(sumx32(sumx16(sum16(lane < 16 ? q0 + q1 : q0))) * (1.0f / D)) + 1e-5f));
    if (lane == 0) st_gran(ws + WS_ST + (size_t)(bid * 8 + wid) * 8, cvtpk(mean, den), tag);
  }
  if (wid < 2) {
    __builtin_amdgcn_s_setprio(3);
    unsigned vg[6];
#pragma unroll
    for (int j = 0; j < 6; ++j) {
      const int u = j * 64 + lane, k = u / 12, pos = u - k * 12;
      vg[j] = k * 3072 + (pos ^ (((k >> 3) & 1) * 2)) * 16 - 1024 * (j & 3);
    }
    const char* xb = x + (size_t)(rb * 128 + wid * 64) * 1280;
    const unsigned lx = ldsb + wid * 8192;
#define LX_ISSUE(ST, SO) do { glds4(vx[0], vx[1], vx[2], vx[3], xb + (ST) * 128, lx + (SO)); glds4(vx[0], vx[1], vx[2], vx[3], xb + (ST) * 128 + 32 * 1280, lx + 4096 + (SO)); } while (0)
    if (has_qkv) {
      const char* wb = wqkv + cb * 256 + (size_t)wid * 32 * 3840;
#define LQ_X(ST) do { kx = NEXT10(kx); LX_ISSUE(kx, ((ST) & 3) * 16384); } while (0)
#define LQ_W(ST)                                                                                                \
  do {                                                                                                          \
    kw = NEXT10(kw);                                                                                            \
    glds4(vw[0], vw[1], vw[2], vw[3], wb + (size_t)kw * (64 * 3840), lx + WR + ((ST) & 3) * 16384);             \
    glds4(vw[0], vw[1], vw[2], vw[3], wb + (size_t)kw * (64 * 3840) + 16 * 3840, lx + WR + 4096 + ((ST) & 3) * 16384); \
  } while (0)
      int kx = sq0, kw = sq0;
      LQ_X(1); LQ_W(1);
      asm volatile("" : "+v"(vg[0]), "+v"(vg[1]), "+v"(vg[2]), "+v"(vg[3]), "+v"(vg[4]), "+v"(vg[5]));
      WVB(17);
      LQ_X(2); LQ_W(2);
      WVB(16);
      LQ_X(3); LQ_W(3); LQ_X(4);
      WVB(24);
      LQ_W(4); LQ_X(5); LQ_W(5);
#pragma unroll 1
      for (int k = 3; k < 7; ++k) {
        WVB(32);
        LQ_X(k + 3); LQ_W(k + 3);
      }
#undef LQ_X
#undef LQ_W
    }
    const char* wb = (wid ? wu : wg) + fbk * 192;
    const unsigned lw = ldsb + wid * 12288;
#define LG_W(J, WO)                                                                                             \
  do {                                                                                                          \
    glds4(vg[0], vg[1], vg[2], vg[3], wb + (size_t)(J) * (64 * 3072), lw + (WO));                               \
    glds2(vg[4], vg[5], wb + (size_t)(J) * (64 * 3072), lw + 4096 + (WO));                                      \
    glds4(vg[0], vg[1], vg[2], vg[3], wb + (size_t)(J) * (64 * 3072) + 32 * 3072, lw + 6144 + (WO));            \
    glds2(vg[4], vg[5], wb + (size_t)(J) * (64 * 3072) + 32 * 3072, lw + 6144 + 4096 + (WO));                   \
  } while (0)
    int kw = sg0, kx = sg0;
    LG_W(kw, GWS);
    if (has_qkv) { WVB(44); WVB(28); WVB(12); }
    LX_ISSUE(kx, 32768); kx = NEXT10(kx);
    LX_ISSUE(kx, 49152); kx = NEXT10(kx);
    kw = NEXT10(kw); LG_W(kw, GW0 - 24576);
    LX_ISSUE(kx, 0); kx = NEXT10(kx);
    int wo = GW0 - 49152;
#pragma unroll 1
    for (int j = 2; j < 9; ++j) {
      WVB(28);
      kw = NEXT10(kw); LG_W(kw, wo);
      LX_ISSUE(kx, ((j + 3) & 3) * 16384); kx = NEXT10(kx);
      wo = wo == GW0 - 49152 ? GW0 : wo - 24576;
    }
    WVB(28);
    kw = NEXT10(kw); LG_W(kw, GW0);
    {
      const char* qg = ws + WS_Q + (size_t)head * QHS + (size_t)(qb * 2 + wid) * QGS;
      glds4_sc1(vl, vl, vl, vl, qg, ldsb + SCR_Q + wid * 5120);
      glds1_sc1(vl, qg + 4096, ldsb + SCR_Q + wid * 5120 + 4096);
    }
#undef LG_W
#undef LX_ISSUE
    WVB(25);
    AT_ISSUE2(0, AT_A);
    AT_ISSUE2(1, AT_B);
    WVB(22);
  } else if (wid < 4) {
    const int hw = wid - 2, ht = hw * 64 + lane;
    const __amdgpu_buffer_rsrc_t rs = __builtin_amdgcn_make_buffer_rsrc(ws, 0, (int)WS_DBG, 0x00020000);
    __builtin_amdgcn_s_setprio(3);
#define HBAR() do { if (has_qkv) { SBAR(); BAR(); SBAR(); } } while (0)
#define PIN5(V) asm volatile("" : "+v"(V[0]), "+v"(V[1]), "+v"(V[2]), "+v"(V[3]), "+v"(V[4]))
    const bf16x8 ones = {0x3f80, 0x3f80, 0x3f80, 0x3f80, 0x3f80, 0x3f80, 0x3f80, 0x3f80};
    const int r = lane & 15, g = lane >> 4;
    const unsigned t32 = (unsigned)tag;
    char* csd = ws + WS_CS + (size_t)((cb * 16 + sq0) * 128 + hw * 64 + (r >> 2) * 16 + 4 * g + (r & 3)) * 8;
    char* cgd = ws + WS_CG + (size_t)((fbk * 16 + sg0) * 192 + hw * 96 + (r >> 2) * 16 + 4 * g + (r & 3)) * 8;
    unsigned sto = (unsigned)WS_ST + (rb * 128 + ht) * 8;
    unsigned cso = (unsigned)WS_CS + ((has_qkv ? cb : 0) * 16 + hh) * 1024 + hw * 512 + r32 * 16;
    unsigned cgo = (unsigned)WS_CG + fbk * 16 * 1536 + hw * 768 + (lane < 48 ? lane : 0) * 16;
    unsigned kpo = (unsigned)WS_KP + (head * 32 + r32) * 32 + hh * 16;
    const int c0 = (head * 20) >> 7, c1 = (head * 20 + 19) >> 7;
    const char* fkv = ws + WS_FQ + ((5 + (g >> 1) * 5 + ((g & 1) ? c1 : c0)) * 16 + r) * 8;
    const char* fq = ws + WS_FQ + (((lane & 1) ? c1 : c0) * 16 + qb * 2 + hw) * 8;
    lds_ptr wqa[4], wga[2];
#pragma unroll
    for (int nt = 0; nt < 4; ++nt) wqa[nt] = lds + WR + (8 * g + (r >> 2)) * 256 + (((hw * 4 + nt) ^ ((r >> 2) | ((g & 1) << 2))) * 32) + (r & 3) * 8;
#pragma unroll
    for (int i = 0; i < 2; ++i) wga[i] = lds + GWS + hw * 12288 + (8 * g + (r >> 2)) * 192 + ((i ^ (g & 1)) * 32) + (r & 3) * 8;
    asm volatile("" : "+v"(csd), "+v"(cgd), "+v"(sto), "+v"(cso), "+v"(cgo), "+v"(kpo), "+v"(fkv), "+v"(fq), "+v"(wqa[0]), "+v"(wqa[1]), "+v"(wqa[2]), "+v"(wqa[3]),
                 "+v"(wga[0]), "+v"(wga[1]));
    if (has_qkv) {
      SBAR(); WVB(1); SBAR();
      f32x4 cs[4];
#pragma unroll
      for (int nt = 0; nt < 4; ++nt) {
        cs[nt] = MFMA16(cat8(ldstr(wqa[nt]), ldstr(wqa[nt] + 1024)), ones, (f32x4{0.f, 0.f, 0.f, 0.f}));
        cs[nt] = MFMA16(cat8(ldstr(wqa[nt] + 8192), ldstr(wqa[nt] + 9216)), ones, cs[nt]);
      }
      const f32x4 v = (r & 8) ? ((r & 4) ? cs[3] : cs[2]) : ((r & 4) ? cs[1] : cs[0]);
      st_gran(csd, f2u((r & 2) ? ((r & 1) ? v[3] : v[2]) : ((r & 1) ? v[1] : v[0])), tag);
      SBAR(); WLB(); SBAR();
      BAR(); BAR(); BAR(); BAR();
    }
    {
      u32x2 gst = bld8_sc1(rs, sto);
      u32x4 gc[5];
      if (has_qkv) {
#pragma unroll
        for (int i = 0; i < 5; ++i) gc[i] = bld16_sc1(rs, cso + i * 2048);
        BAR(); BAR();
      }
      _Pragma("unroll 1") for (unsigned spins = 0; spins < kSpinCap; ++spins) {
        unsigned bad = gst[1] ^ t32;
        if (has_qkv) {
#pragma unroll
          for (int i = 0; i < 5; ++i) bad |= (gc[i][1] ^ t32) | (gc[i][3] ^ t32);
        }
        if (wall(bad == 0u)) break;
        __builtin_amdgcn_s_sleep(2);
        asm volatile("" ::: "memory");
        gst = bld8_sc1(rs, sto);
        if (has_qkv) {
#pragma unroll
          for (int i = 0; i < 5; ++i) gc[i] = bld16_sc1(rs, cso + i * 2048);
        }
      }
      HBAR();
      *(LAS f32x2*)(lds + SCR_STAT + ht * 8) = f32x2{bflo(gst[0]), 1.0f / bfhi(gst[0])};
      if (has_qkv) {
        float a = 0.f, b = 0.f;
#pragma unroll
        for (int i = 0; i < 5; ++i) { a = adds(a, u2f(gc[i][0])); b = adds(b, u2f(gc[i][2])); }
        a = sumx32(a); b = sumx32(b);
        if (lane < 32) *(LAS f32x2*)(lds + SCR_CS + hw * 256 + lane * 8) = f32x2{a, b};
        WLB();
      }
    }
    BAR();
    {
      f32x4 cs[6];
#pragma unroll
      for (int nt = 0; nt < 6; ++nt) {
        const lds_ptr wa = wga[nt & 1] + (nt >> 1) * 64;
        cs[nt] = MFMA16(cat8(ldstr(wa), ldstr(wa + 768)), ones, (f32x4{0.f, 0.f, 0.f, 0.f}));
        cs[nt] = MFMA16(cat8(ldstr(wa + 6144), ldstr(wa + 6912)), ones, cs[nt]);
      }
      asm volatile("" : "+v"(cs[0]), "+v"(cs[1]), "+v"(cs[2]), "+v"(cs[3]), "+v"(cs[4]), "+v"(cs[5]));
      SBAR(); WLB(); SBAR();
      const f32x4 v = (r & 8) ? ((r & 4) ? cs[3] : cs[2]) : ((r & 4) ? cs[1] : cs[0]), w = (r & 4) ? cs[5] : cs[4];
      st_gran(cgd, f2u((r & 2) ? ((r & 1) ? v[3] : v[2]) : ((r & 1) ? v[1] : v[0])), tag);
      if (r < 8) st_gran(cgd + 512, f2u((r & 2) ? ((r & 1) ? w[3] : w[2]) : ((r & 1) ? w[1] : w[0])), tag);
    }
    BAR();
    if (ht == 0 && has_qkv) set_flag(ws + WS_FQ + (cb * 16 + rb) * 8, tag);
    BAR(); BAR(); BAR();
    {
      const u64 f0 = __hip_atomic_load((gu64*)fkv, RLX_AGENT), f1 = __hip_atomic_load((gu64*)fq, RLX_AGENT);
      BAR();
      if (!wall(f0 == tag && f1 == tag)) wait_tag(fkv, fq, tag);
      __builtin_amdgcn_fence(__ATOMIC_ACQUIRE, "wavefront");
    }
    {
      const u32x4 kp = bld16_sc1(rs, kpo);
      u32x4 gc[10];
#pragma unroll
      for (int i = 0; i < 10; ++i) gc[i] = bld16_sc1(rs, cgo + i * 1536);
      BAR(); BAR();
      const float ks = hh ? u2f(kp[0]) : adds(adds(u2f(kp[0]), u2f(kp[1])), adds(u2f(kp[2]), u2f(kp[3])));
      const float kn2 = maxx16(max16(sumx32(ks)));
      if (ht == 0) *(LAS float*)(lds + SCR_KN) = kn2;
      _Pragma("unroll 1") for (unsigned spins = 0; spins < kSpinCap; ++spins) {
        unsigned bad = 0u;
#pragma unroll
        for (int i = 0; i < 10; ++i) bad |= (gc[i][1] ^ t32) | (gc[i][3] ^ t32);
        if (wall(bad == 0u)) break;
        __builtin_amdgcn_s_sleep(2);
        asm volatile("" ::: "memory");
#pragma unroll
        for (int i = 0; i < 10; ++i) gc[i] = bld16_sc1(rs, cgo + i * 1536);
      }
      SBAR(); WLB(); SBAR();
      float a = 0.f, b = 0.f;
#pragma unroll
      for (int i = 0; i < 10; ++i) { a = adds(a, u2f(gc[i][0])); b = adds(b, u2f(gc[i][2])); }
      if (lane < 48) *(LAS f32x2*)(lds + SCR_CG + hw * 384 + lane * 8) = f32x2{a, b};
    }
  } else {
    const int cw = wid - 4;
    __builtin_amdgcn_s_setprio(2);
    if (has_qkv) {
      if (cb < 10) qkv_compute<true>(lds, cw, lane, rb, cb, ws TPASS);
      else qkv_compute<false>(lds, cw, lane, rb, cb, ws TPASS);
    }
    gu_compute(lds, cw, lane, rb, fbk, ws TPASS);
    __builtin_amdgcn_s_setprio(0);
  }
  {
    const lds_ptr qp = lds + SCR_Q + ((wid >> 1) & 1) * 5120 + (wid & 1) * 512 + r32 * 16;
    const u32x4 l0 = __builtin_bit_cast(u32x4, lds128(qp + hh * 2048)), l1 = __builtin_bit_cast(u32x4, lds128(qp + hh * 2048 + 1024));
    const u32x4 qbb = __builtin_bit_cast(u32x4, lds128(qp + 4096));
#pragma unroll
    for (int a = 0; a < 2; ++a) {
      qa[a] = __builtin_bit_cast(bf16x8, u32x4{l0[2 * a], l0[2 * a + 1], l1[2 * a], l1[2 * a + 1]});
#pragma unroll
      for (int j = 0; j < 2; ++j) { u32x2 u = {hh == j ? qbb[2 * a] : 0u, hh == j ? qbb[2 * a + 1] : 0u}; qv[a][j] = __builtin_bit_cast(s16x4, u); }
    }
    LAS f32x2* mp = (LAS f32x2*)(lds + SCR_M + (wid & 3) * 256 + r32 * 8);
    if (wid < 4) {
      const float kn2 = *(const LAS float*)(lds + SCR_KN);
#pragma unroll
      for (int a = 0; a < 2; ++a) {
        const unsigned w[5] = {hh ? qbb[2 * a + 1] : qbb[2 * a], l0[2 * a], l0[2 * a + 1], l1[2 * a], l1[2 * a + 1]};
        float ss = 0.f;
#pragma unroll
        for (int e = 0; e < 5; ++e) { ss = fmas(bflo(w[e]), bflo(w[e]), ss); ss = fmas(bfhi(w[e]), bfhi(w[e]), ss); }
        mrow[a] = __builtin_amdgcn_sqrtf(fmas(sumx32(ss), kn2 * 1.04f, 0.f));
      }
      *mp = f32x2{mrow[0], mrow[1]};
      WVB(11);
      __builtin_amdgcn_s_setprio(0);
    } else {
      const f32x2 m = *mp;
      mrow[0] = m[0]; mrow[1] = m[1];
    }
  }

  const int kh = wid >> 2;
#define D_ISSUE(ST, XP, XS)                                                                                    \
  do {                                                                                                         \
    const unsigned d_ = rfl(dl0 + ((ST) & 3) * D_STG);                                                         \
    glds4_sc1(vl, vl + (XS) - 1024, vl + 2 * ((XS) - 1024), vl + 3 * ((XS) - 1024), XP, d_);                   \
    glds4_sc1(vl, vl, vl, vl, wpb + (size_t)(ST) * 20480, d_ + 4096);                                          \
    glds1_sc1(vl, wpb + (size_t)(ST) * 20480 + 4096, d_ + 8192);                                               \
  } while (0)
#define D_ISSUE_H(ST) D_ISSUE(ST, hb + (ST) * 4096, 192 * 256)
#define FRESH()                                                                                                \
  int lane_ = lane, bid_ = bid, wq = wid & 3; char* wz = ws;                                                   \
  asm volatile("" : "+v"(lane_), "+s"(bid_), "+s"(wz), "+s"(wq));                                              \
  const int xc = bid_ & 7, ix = bid_ >> 3, mb = (xc >> 1) * 8 + (ix & 7), nb = (xc & 1) * 4 + (ix >> 3);       \
  const unsigned vl = lane_ * 16, dl0 = ldsb + wq * 9216;                                                      \
  const char* wpb = wz + WS_WP + (size_t)(nb * 68 + wq) * 5120;                                                \
  const char* hb = wz + WS_H + (size_t)mb * 4 * 192 * 256 + wq * 1024;                                         \
  (void)vl; (void)dl0; (void)wpb; (void)hb
  {
    const int dsel = r32 <= 20 ? r32 : (r32 < 28 ? 20 : 4);
    const int koff = kh * (int)KVGS + hh * 2048 + r32 * 16, kboff = kh * (int)KVGS + 4096 + lane * 16, voff = kh * (int)KVGS + 5120 + hh * 336 + dsel * 16;
    for (int pass = 0;; ++pass) {
      f32x16 negm[2], sc[2], oacc[2];
#pragma unroll
      for (int a = 0; a < 2; ++a)
#pragma unroll
        for (int e = 0; e < 16; ++e) { negm[a][e] = -mrow[a]; oacc[a][e] = 0.f; }
      asm volatile("" : "+v"(negm[0]), "+v"(negm[1]));
      u32x4 pw[2][2] = {};
      bf16x8 ka, kb, vf[2] = {};
      int sl_cur = AT_A, sl_next = AT_B, sl_free = AT_C;
#define LDKA(P, t) cat8(lds64((P) + ((t) >> 1) * 512 + ((t) & 1) * 8), lds64((P) + 1024 + ((t) >> 1) * 512 + ((t) & 1) * 8))
      ka = LDKA(lds + koff + AT_A, 0);
      kb = lds128(lds + kboff + AT_A);
#define KBH(t) (((t) & 1) ? __builtin_shufflevector(kb, kb, 4, 5, 6, 7) : __builtin_shufflevector(kb, kb, 0, 1, 2, 3))
      sc[0] = MFMA32(ka, qa[0], negm[0]);
      sc[0] = MFMA32K8(KBH(0), qv[0][0], sc[0]);
#define EX4(X, i) do { X[i] = __builtin_amdgcn_exp2f(X[i]); X[i + 1] = __builtin_amdgcn_exp2f(X[i + 1]); X[i + 2] = __builtin_amdgcn_exp2f(X[i + 2]); X[i + 3] = __builtin_amdgcn_exp2f(X[i + 3]); } while (0)
#define PK2(P, j, X, i) do { P[j] = cvtpk(X[i], X[i + 1]); P[j + 1] = cvtpk(X[i + 2], X[i + 3]); } while (0)
#define PFR(a, s) __builtin_bit_cast(bf16x8, pw[a][s])
#define UNIT(A, B, TB, LDK, LDV)                                                             \
  do {                                                                                       \
    sc[B] = MFMA32(ka, qa[B], negm[B]); SBAR();                                              \
    EX4(sc[A], 0); PIN(sc[A]); SBAR();                                                       \
    sc[B] = MFMA32K8(KBH(TB), qv[B][(TB) >> 1], sc[B]); SBAR();                              \
    LDK; SBAR();                                                                             \
    EX4(sc[A], 4); PK2(pw[A][0], 0, sc[A], 0); PIN(sc[A]); PIN(pw[A][0]); SBAR();            \
    oacc[B] = MFMA32(vf[0], PFR(B, 0), oacc[B]); SBAR();                                     \
    EX4(sc[A], 8); PK2(pw[A][0], 2, sc[A], 4); PIN(sc[A]); PIN(pw[A][0]); SBAR();            \
    oacc[B] = MFMA32(vf[1], PFR(B, 1), oacc[B]); SBAR();                                     \
    LDV; SBAR();                                                                             \
    EX4(sc[A], 12); PK2(pw[A][1], 0, sc[A], 8); PK2(pw[A][1], 2, sc[A], 12); PIN(pw[A][1]); SBAR(); \
  } while (0)
#define LDV_(t) do { vf[0] = lds128(vp + (t) * 1344); vf[1] = lds128(vp + (t) * 1344 + 672); } while (0)
      for (int c = 0; c < 8; ++c) {
        lds_ptr kp_ = lds + koff + sl_cur, vp = lds + voff + sl_cur, kn = lds + koff + sl_next, kbn = lds + kboff + sl_next;
        UNIT(0, 1, 0, ka = LDKA(kp_, 1), LDV_(0));
        UNIT(1, 0, 1, (void)0, (void)0);
        UNIT(0, 1, 1, ka = LDKA(kp_, 2), LDV_(1));
        UNIT(1, 0, 2, (void)0, (void)0);
        UNIT(0, 1, 2, ka = LDKA(kp_, 3), LDV_(2));
        UNIT(1, 0, 3, (void)0, (void)0);
        WVB(0);
        if (c == 4 || c == 5) sl_free = AT_H + (c - 4) * AT_CH;
        if (wid < 4) {
          if (c < 6) AT_ISSUE(c + 2, sl_free);
          if (pass == 0 || c == 6) {
            FRESH();
            if (c == 0) {
              if (wq == 0 && lane_ == 0) {
                const int t_ = (xc & 1) * 30 + ix;
                set_flag(wz + WS_FH + (((xc >> 1) * 4 + (t_ & 3)) * 16 + (xc & 1) * 8 + (ix >> 2)) * 8, tag);
              }
              if (bid_ < 136) {
                const int ks = bid_ >> 1;
                unsigned v[5];
#pragma unroll
                for (int j = 0; j < 5; ++j) {
                  const int u = j * 64 + lane_, pr = u / 10, pos = u - pr * 10, k = pr ^ (((pr >> 3) & 1) << 2);
                  const int row = ks < 48 ? k : ks < 64 ? (k >> 4) * 20 + (k & 15) : (k >> 2) * 20 + 16 + (k & 3);
                  v[j] = row * 1280 + pos * 16 - 1024 * (j & 3);
                }
                const char* src = ks < 48 ? wd + (size_t)ks * 32 * 1280 : ks < 64 ? wo + (size_t)(ks - 48) * 40 * 1280 : wo + (size_t)(ks - 64) * 160 * 1280;
                glds5(v, src + ((bid_ & 1) * 4 + wq) * 160, rfl(ldsb + AT_RP + wq * 5120));
              }
            } else if (c == 1) {
              if (bid_ < 136) {
                u32x4 t[5];
#pragma unroll
                for (int j = 0; j < 5; ++j) t[j] = __builtin_bit_cast(u32x4, lds128(lds + AT_RP + wq * 5120 + j * 1024 + vl));
#pragma unroll
                for (int j = 0; j < 5; ++j) st16_sc1(wz + WS_WP + (size_t)(((bid_ & 1) * 4 + wq) * 68 + (bid_ >> 1)) * 5120, vl + j * 1024, t[j]);
              }
            } else if (c == 2) {
              if (bid_ < 136 && wq == 0 && lane_ == 0) set_flag(wz + WS_FW + ((bid_ & 1) * 128 + (bid_ >> 1)) * 8, tag);
            } else if (c == 3) {
              glds1_sc1((unsigned)WS_FH + (xc >> 1) * 512 + (lane_ & 31) * 16, wz, rfl(ldsb + AT_RP + 1024 + wq * 2048));
              glds1_sc1((unsigned)WS_FW + (xc & 1) * 1024 + (lane_ < 34 ? lane_ : 0) * 16, wz, rfl(ldsb + AT_RP + 2048 + wq * 2048));
            } else if (c == 4) {
              const u32x4 f0 = __builtin_bit_cast(u32x4, lds128(lds + AT_RP + 1024 + wq * 2048 + vl));
              const u32x4 f1 = __builtin_bit_cast(u32x4, lds128(lds + AT_RP + 2048 + wq * 2048 + vl));
              const unsigned tl = (unsigned)tag, th = (unsigned)(tag >> 32);
              if (!wall(f0[0] == tl && f0[1] == th && f0[2] == tl && f0[3] == th && f1[0] == tl && f1[1] == th && f1[2] == tl && f1[3] == th)) {
                wait_tag(wz + WS_FH + ((xc >> 1) * 64 + lane_) * 8, wz + WS_FW + ((xc & 1) * 128 + lane_) * 8, tag);
                wait_tag(wz + WS_FW + ((xc & 1) * 128 + 64 + (lane_ & 3)) * 8, tag);
              }
              const unsigned lt = (wq * 64 + lane_) * 128;
              const char* hp = wz + WS_H + (size_t)((xc >> 1) * 32 + ix) * 49152;
              touch_sc1(lt, hp, rfl(ldsb + AT_RP));
              if (wq < 2) touch_sc1(lt, hp + 32768, rfl(ldsb + AT_RP));
            } else if (c == 5) {
              const unsigned lt = (wq * 64 + lane_) * 128;
              const char* wp = wz + WS_WP + (size_t)(xc & 1) * 4 * 68 * 5120 + (size_t)ix * 43520;
              touch_sc1(lt, wp, rfl(ldsb + AT_RP));
              if (wq < 2) touch_sc1(lt < 43520 - 32768 ? lt : 0u, wp + 32768, rfl(ldsb + AT_RP));
            } else if (c == 6) {
              D_ISSUE_H(0); D_ISSUE_H(1); D_ISSUE_H(2);
            }
          }
        }
        SBAR();
        UNIT(0, 1, 3, do { ka = LDKA(kn, 0); kb = lds128(kbn); } while (0), LDV_(3));
        UNIT(1, 0, 0, (void)0, (void)0);
        const int t_ = sl_cur; sl_cur = sl_next; sl_next = sl_free; sl_free = t_;
      }
      oacc[1] = MFMA32(vf[0], PFR(1, 0), oacc[1]);
      oacc[1] = MFMA32(vf[1], PFR(1, 1), oacc[1]);
      {
        LAS f32x4* ex = (LAS f32x4*)(lds + AT_EX + wid * 3072) + lane;
        const f32x16 o = kh ? oacc[0] : oacc[1];
        ex[0] = f32x4{o[0], o[1], o[2], o[3]}; ex[64] = f32x4{o[4], o[5], o[6], o[7]}; ex[128] = f32x4{o[8], o[9], o[10], o[11]};
      }
      WLB();
      const LAS f32x4* in = (const LAS f32x4*)(lds + AT_EX + (wid ^ 4) * 3072) + lane;
      const f32x16 o = kh ? oacc[1] : oacc[0];
      const f32x4 o0 = in[0] + f32x4{o[0], o[1], o[2], o[3]}, o1 = in[64] + f32x4{o[4], o[5], o[6], o[7]}, o2 = in[128] + f32x4{o[8], o[9], o[10], o[11]};
      auto sw = __builtin_amdgcn_permlane32_swap(f2u(o2[0]), f2u(o2[0]), false, false);
      const float ls = u2f(sw[1]);
      const float rl = __builtin_amdgcn_rcpf(ls);
      {
        FRESH();
        const int head = xc * 4 + (ix >> 3), qb = ix & 7, r32 = lane_ & 31, hh = lane_ >> 5;
        const unsigned w0 = cvtpk(o0[0] * rl, o0[1] * rl), w1 = cvtpk(o0[2] * rl, o0[3] * rl), w2 = cvtpk(o1[0] * rl, o1[1] * rl), w3 = cvtpk(o1[2] * rl, o1[3] * rl);
        auto s0 = __builtin_amdgcn_permlane32_swap(w0, w2, false, false);
        auto s1 = __builtin_amdgcn_permlane32_swap(w1, w3, false, false);
        const int tok = qb * 256 + wq * 64 + kh * 32 + r32;
        st16_sc1(wz + WS_O, (((tok >> 4) * 80 + head * 2 + hh) * 16 + (tok & 15)) * 16, u32x4{s0[0], s1[0], s0[1], s1[1]});
        if (hh == 0)
          st8_sc1(wz + WS_O + ((size_t)((tok >> 4) * 80 + 64 + (head >> 1)) * 16 + (tok & 15)) * 16 + (head & 1) * 8,
                  u32x2{cvtpk(o2[0] * rl, o2[1] * rl), cvtpk(o2[2] * rl, o2[3] * rl)});
      }
      const bool anybad = wany(!(ls >= 0x1p-40f && ls <= 0x1p40f));
      if (lane == 0) *(LAS unsigned*)(lds + SCR + 1024 + wid * 4) = (pass == 0 && anybad) ? 1u : 0u;
      WLB();
      const u32x4 fa = *(const LAS u32x4*)(lds + SCR + 1024), fb = *(const LAS u32x4*)(lds + SCR + 1024 + 16);
      if (!rfl(fa[0] | fa[1] | fa[2] | fa[3] | fb[0] | fb[1] | fb[2] | fb[3])) break;
      if (wid < 4) { AT_ISSUE(0, AT_A); AT_ISSUE(1, AT_B); }
      float mx[2] = {-INFINITY, -INFINITY};
      int lane = threadIdx.x & 63;
      PIN(lane);
      const int r32 = lane & 31, hh = lane >> 5;
      for (int gi = 0; gi < 16; ++gi) {
        const char* gp = kvg + (size_t)gi * KVGS;
        kb = __builtin_bit_cast(bf16x8, ld16_sc1_wait(gp + 4096 + lane * 16));
#pragma unroll
        for (int tp = 0; tp < 2; ++tp) {
          const u32x4 k0 = ld16_sc1_wait(gp + hh * 2048 + r32 * 16 + tp * 512), k1 = ld16_sc1_wait(gp + hh * 2048 + r32 * 16 + 1024 + tp * 512);
#pragma unroll
          for (int u = 0; u < 2; ++u) {
            ka = __builtin_bit_cast(bf16x8, u32x4{k0[2 * u], k0[2 * u + 1], k1[2 * u], k1[2 * u + 1]});
#pragma unroll
            for (int a = 0; a < 2; ++a) {
              f32x16 s = MFMA32(ka, qa[a], f32x16{});
              s = MFMA32K8(KBH(u), qv[a][tp], s);
#pragma unroll
              for (int e = 0; e < 16; ++e) mx[a] = fmaxf(mx[a], s[e]);
            }
          }
        }
      }
#pragma unroll
      for (int a = 0; a < 2; ++a) mrow[a] = maxx32(mx[a]);
      WVB(0);
    }
  }
#undef AT_ISSUE
#undef UNIT

  {
  FRESH();
  if (wid < 4) {
    __builtin_amdgcn_s_setprio(3);
    D_ISSUE_H(3);
    const char* ob = wz + WS_O + (size_t)mb * 4 * 80 * 256 + wq * 1024;
    BAR();
    D_ISSUE_H(4);
    WVB(18);
    if (wq == 0 && lane_ == 0) set_flag(wz + WS_FO + ((ix & 7) * 32 + xc * 4 + (ix >> 3)) * 8, tag);
    D_ISSUE_H(5);
#pragma unroll 1
    for (int k = 3; k <= 8; ++k) { WVB(18); D_ISSUE_H(k + 3); }
#pragma unroll 1
    for (int j = 0; j < 5; ++j) { WVB(18); D_ISSUE(12 + j, ob + j * 4096, 80 * 256); }
    WVB(18);
    WVB(9);
    WVB(0);
    if (bid_ == 0 && wq == 0) {
#pragma unroll 1
      for (int i = 0; i < 4; ++i) wait_tag(wz + WS_FH + (i * 64 + lane_) * 8, tag);
      if (lane_ == 0) set_flag(wz + WS_EP, tag);
    }
    BAR();
  } else {
    const int r = lane_ & 15, g = lane_ >> 4;
    lds_ptr xa[4];
#pragma unroll
    for (int i = 0; i < 4; ++i) xa[i] = lds + wq * 9216 + (i ^ wq) * 1024 + vl;
    lds_ptr wa0 = lds + wq * 9216 + 4096 + (8 * g + ((r >> 2) ^ (4 * (g & 1)))) * 160 + (r & 3) * 8;
    lds_ptr wa1 = lds + wq * 9216 + 4096 + (8 * g + ((4 + (r >> 2)) ^ (4 * (g & 1)))) * 160 + (r & 3) * 8;
    f32x4 acc[5][4];
#pragma unroll
    for (int i = 0; i < 5; ++i)
#pragma unroll
      for (int j = 0; j < 4; ++j) acc[i][j] = f32x4{0.f, 0.f, 0.f, 0.f};
    bf16x8 xf[2][4], wf[2][5];
#define D_READ(B, SO)                                                                                  \
  do {                                                                                                 \
    _Pragma("unroll") for (int i = 0; i < 5; ++i) {                                                    \
      wf[B][i] = cat8(ldstr(wa0 + (SO) + i * 32), ldstr(wa1 + (SO) + i * 32));                         \
      if (i < 4) xf[B][i] = lds128(xa[i] + (SO));                                                      \
    }                                                                                                  \
  } while (0)
#define D_MMA(B)                                                                                       \
  do {                                                                                                 \
    _Pragma("unroll") for (int nt = 0; nt < 5; ++nt)                                                   \
      _Pragma("unroll") for (int mt = 0; mt < 4; ++mt) acc[nt][mt] = MFMA16(wf[B][nt], xf[B][mt], acc[nt][mt]); \
  } while (0)
#define D_MIX() do { _Pragma("unroll") for (int i = 0; i < 14; ++i) { SGB(0x8, 1); SGB(0x100, 1); } SGB(0x8, 6); } while (0)
    D_READ(0, 0);
    int so = D_STG;
    const char* fop = wz + WS_FO + (mb >> 2) * 256;
#pragma unroll 1
    for (int st = 0; st < 16; st += 2) {
      if (st == 8) {
        asm volatile("s_waitcnt vmcnt(0)" ::: "memory");
        const u32x4 f = __builtin_bit_cast(u32x4, lds128(lds + SCR + 2048 + wq * 1024 + vl));
        const unsigned tl = (unsigned)tag, th = (unsigned)(tag >> 32);
        if (!wall(f[0] == tl && f[1] == th && f[2] == tl && f[3] == th)) wait_tag(fop + (lane_ & 31) * 8, tag);
      }
      WLB();
      SBAR();
      D_READ(1, so); D_MMA(0); D_MIX();
      so = so == 3 * D_STG ? 0 : so + D_STG;
      SBAR();
      if (st == 0) asm volatile("s_waitcnt vmcnt(0)" ::: "memory");
      if (st == 6) glds1_sc1(r * 16, fop, rfl(ldsb + SCR + 2048 + wq * 1024));
      WLB();
      SBAR();
      D_READ(0, so); D_MMA(1); D_MIX();
      so = so == 3 * D_STG ? 0 : so + D_STG;
      SBAR();
    }
    const unsigned oo = ((mb * 64 + wq * 16 + r) * 640 + nb * 80 + 4 * g) * 2;
    u32x2 xres[5];
#pragma unroll
    for (int nt = 0; nt < 5; ++nt) xres[nt] = *(const u32x2*)(x + oo + nt * 32);
#pragma unroll
    for (int mt = 3; mt >= 0; --mt)
#pragma unroll
      for (int nt = 0; nt < 5; ++nt) {
        acc[nt][mt] = MFMA16(wf[0][nt], xf[0][mt], acc[nt][mt]);
      }
#pragma unroll
    for (int mt = 3; mt >= 1; --mt) {
      LAS f32x4* ex = (LAS f32x4*)(lds + 40960 + ((mt ^ wq) * 3 + mt - 1) * 5120 + vl);
#pragma unroll
      for (int nt = 0; nt < 5; ++nt) ex[nt * 64] = acc[nt][mt];
    }
    WLB();
    const LAS f32x4* in = (const LAS f32x4*)(lds + 40960 + wq * 15360 + vl);
#pragma unroll
    for (int nt = 0; nt < 5; ++nt) {
      const f32x4 a = (acc[nt][0] + in[nt * 64]) + (in[320 + nt * 64] + in[640 + nt * 64]);
      u32x2 o = {cvtpk(bflo(xres[nt][0]) + a[0], bfhi(xres[nt][0]) + a[1]), cvtpk(bflo(xres[nt][1]) + a[2], bfhi(xres[nt][1]) + a[3])};
      *(u32x2*)(out + oo + nt * 32) = o;
    }
  }
  }
}

extern "C" void kernel_launch(void* const* d_in, const int* in_sizes, int n_in, void* d_out, int out_size, void* d_ws, size_t ws_size,
                              hipStream_t stream) {
  fused_block<<<256, 512, 0, stream>>>((const char*)d_in[0], (const char*)d_in[1], (const char*)d_in[2], (const char*)d_in[4],
                                       (const char*)d_in[5], (const char*)d_in[6], (char*)d_ws, (char*)d_out);
}
```

```cpp
#include <hip/hip_runtime.h>
#include <hip/hip_bf16.h>
#include <stdint.h>

#define DI __device__ __forceinline__
#define LAS __attribute__((address_space(3)))
#define GAS __attribute__((address_space(1)))
typedef short bf16x8 __attribute__((ext_vector_type(8)));
typedef short s16x4 __attribute__((ext_vector_type(4)));
typedef float f32x4 __attribute__((ext_vector_type(4)));
typedef float f32x2 __attribute__((ext_vector_type(2)));
typedef float f32x16 __attribute__((ext_vector_type(16)));
typedef unsigned u32x4 __attribute__((ext_vector_type(4)));
typedef unsigned u32x2 __attribute__((ext_vector_type(2)));
typedef __bf16 bf16x2_t __attribute__((ext_vector_type(2)));
typedef LAS char* lds_ptr;
typedef unsigned long long u64;
typedef GAS u64 gu64;

constexpr int S = 2048, D = 640, F = 1536;
constexpr size_t QGS = 5120, QHS = 16 * QGS;
constexpr size_t KVGS = 10496, KVHS = 16 * KVGS;
constexpr size_t WS_EP = 0;
constexpr size_t WS_FQ = 4096;
constexpr size_t WS_FH = WS_FQ + 2048;
constexpr size_t WS_FO = WS_FH + 2048;
constexpr size_t WS_FW = WS_FO + 2048;
constexpr size_t WS_ST = 16384;
constexpr size_t WS_CG = WS_ST + 2048 * 8;
constexpr size_t WS_KP = WS_CG + 16 * 3072 * 8;
constexpr size_t WS_CS = WS_KP + 32 * 32 * 8 * 4;
constexpr size_t WS_Q = 1048576;
constexpr size_t WS_KV = WS_Q + 32 * QHS;
constexpr size_t WS_H = WS_KV + 32 * KVHS + 4096;
constexpr size_t WS_O = WS_H + (size_t)S * F * 2;
constexpr size_t WS_WP = WS_O + (size_t)S * D * 2;
static_assert(WS_CS + 15 * 16 * 128 * 8 <= WS_Q, "ws map");

#define SBAR() __builtin_amdgcn_sched_barrier(0)
#define PIN(x) asm volatile("" : "+v"(x))
constexpr size_t WS_DBG = (size_t)128 << 20;
#define TS() do {} while (0)
#define TARGS
#define TPASS
#define WVB(N) wait_vm_bar_<N>()
#define WLB() wait_lgkm_bar_()
#define BAR() bar_()
#define MFMA16(a, b, c) __builtin_amdgcn_mfma_f32_16x16x32_bf16(a, b, c, 0, 0, 0)
#define MFMA32(a, b, c) __builtin_amdgcn_mfma_f32_32x32x16_bf16(a, b, c, 0, 0, 0)
#define MFMA32K8(a, b, c) __builtin_amdgcn_mfma_f32_32x32x8bf16_1k(a, b, c, 0, 0, 0)
#define RLX_AGENT __ATOMIC_RELAXED, __HIP_MEMORY_SCOPE_AGENT
#define NOPK 0
#define CODE_LINES 264
#define DOSE 0
constexpr unsigned kSpinCap = 1u << 17;
constexpr int LDS_BYTES = 163840;

DI unsigned f2u(float x) { return __builtin_bit_cast(unsigned, x); }
DI float u2f(unsigned x) { return __builtin_bit_cast(float, x); }
DI bool wall(bool p) { return __builtin_amdgcn_ballot_w64(!p) == 0; }
DI bool wany(bool p) { return __builtin_amdgcn_ballot_w64(p) != 0; }
DI float adds(float a, float b) { float r; asm("v_add_f32 %0, %1, %2" : "=v"(r) : "v"(a), "v"(b)); return r; }
DI float fmas(float a, float b, float c) { float r; asm("v_fma_f32 %0, %1, %2, %3" : "=v"(r) : "v"(a), "v"(b), "v"(c)); return r; }
DI unsigned rfl(unsigned x) { return __builtin_amdgcn_readfirstlane(x); }
DI unsigned cvtpk(float lo, float hi) { f32x2 v = {lo, hi}; bf16x2_t b = __builtin_convertvector(v, bf16x2_t); return __builtin_bit_cast(unsigned, b); }
DI float bfr(float x) { return u2f(cvtpk(x, 0.f) << 16); }
DI float bflo(unsigned u) { return u2f(u << 16); }
DI float bfhi(unsigned u) { return u2f(u & 0xffff0000u); }
DI bf16x8 lds128(lds_ptr p) { return *(const LAS bf16x8*)p; }
DI s16x4 lds64(lds_ptr p) { return *(const LAS s16x4*)p; }
DI s16x4 ldstr(lds_ptr p) { return __builtin_amdgcn_ds_read_tr16_b64_v4i16((LAS s16x4*)p); }
DI bf16x8 cat8(s16x4 a, s16x4 b) { return __builtin_shufflevector(a, b, 0, 1, 2, 3, 4, 5, 6, 7); }
template <int N> DI void wait_vm_bar_() { asm volatile("s_waitcnt vmcnt(%0) lgkmcnt(0)\n\ts_barrier" ::"i"(N) : "memory"); }
DI void wait_lgkm_bar_() { asm volatile("s_waitcnt lgkmcnt(0)\n\ts_barrier" ::: "memory"); }
DI void st16_sc1(char* p, u32x4 v) { asm volatile("global_store_dwordx4 %0, %1, off sc1\n\ts_nop 1" ::"v"(p), "v"(v) : "memory"); }
DI void st16_sc1(const char* sb, unsigned off, u32x4 v) { asm volatile("global_store_dwordx4 %0, %1, %2 sc1\n\ts_nop 1" ::"v"(off), "v"(v), "s"(sb) : "memory"); }
DI void st8_sc1(char* p, u32x2 v) { asm volatile("global_store_dwordx2 %0, %1, off sc1\n\ts_nop 1" ::"v"(p), "v"(v) : "memory"); }
DI u32x4 ld16_sc1_wait(const char* p) { u32x4 v; asm volatile("global_load_dwordx4 %0, %1, off sc1\n\ts_waitcnt vmcnt(0)" : "=&v"(v) : "v"(p) : "memory"); return v; }

DI void glds2(unsigned v0, unsigned v1, const void* sbase, unsigned m0v) {
  unsigned sv;
  asm volatile("s_mov_b32 %0, m0\n\ts_mov_b32 m0, %4\n\ts_nop 0\n\t"
               "global_load_lds_dwordx4 %1, %3\n\t"
               "global_load_lds_dwordx4 %2, %3 offset:1024\n\t"
               "s_mov_b32 m0, %0"
               : "=&s"(sv) : "v"(v0), "v"(v1), "s"(sbase), "s"(m0v) : "memory");
}
DI void glds5(const unsigned (&v)[5], const void* sbase, unsigned m0v) {
  unsigned sv;
  asm volatile("s_waitcnt lgkmcnt(0)\n\ts_mov_b32 %0, m0\n\ts_mov_b32 m0, %7\n\ts_nop 0\n\t"
               "global_load_lds_dwordx4 %1, %6\n\t"
               "global_load_lds_dwordx4 %2, %6 offset:1024\n\t"
               "global_load_lds_dwordx4 %3, %6 offset:2048\n\t"
               "global_load_lds_dwordx4 %4, %6 offset:3072\n\t"
               "s_add_u32 m0, m0, 4096\n\ts_nop 0\n\t"
               "global_load_lds_dwordx4 %5, %6\n\t"
               "s_mov_b32 m0, %0"
               : "=&s"(sv) : "v"(v[0]), "v"(v[1]), "v"(v[2]), "v"(v[3]), "v"(v[4]), "s"(sbase), "s"(m0v) : "memory", "scc");
}
DI void wait_tag(const char* p0, const char* p1, u64 tag) {
  _Pragma("unroll 1") for (unsigned spins = 0; spins < kSpinCap; ++spins) {
    const u64 a = __hip_atomic_load((gu64*)p0, RLX_AGENT), b = __hip_atomic_load((gu64*)p1, RLX_AGENT);
    if (wall(a == tag && b == tag)) break;
    __builtin_amdgcn_s_sleep(2);
  }
  __builtin_amdgcn_fence(__ATOMIC_ACQUIRE, "wavefront");
}
DI void wait_tag(const char* p0, u64 tag) {
  _Pragma("unroll 1") for (unsigned spins = 0; spins < kSpinCap; ++spins) {
    const u64 a = __hip_atomic_load((gu64*)p0, RLX_AGENT);
    if (wall(a == tag)) break;
    __builtin_amdgcn_s_sleep(2);
  }
  __builtin_amdgcn_fence(__ATOMIC_ACQUIRE, "wavefront");
}
DI void set_flag(char* p, u64 tag) { __hip_atomic_store((gu64*)p, tag, RLX_AGENT); }
DI u64 make_tag(u64 e) {
  const u64 u = ((u64)rfl((unsigned)(e >> 32)) << 32) | rfl((unsigned)e);
  return u * 0x9E3779B97F4A7C15ull + 0xD1B54A32D192ED03ull;
}

DI void glds4(unsigned v0, unsigned v1, unsigned v2, unsigned v3, const void* sbase, unsigned m0v) {
  unsigned sv;
  asm volatile("s_mov_b32 %0, m0\n\ts_mov_b32 m0, %6\n\ts_nop 0\n\t"
               "global_load_lds_dwordx4 %1, %5\n\t"
               "global_load_lds_dwordx4 %2, %5 offset:1024\n\t"
               "global_load_lds_dwordx4 %3, %5 offset:2048\n\t"
               "global_load_lds_dwordx4 %4, %5 offset:3072\n\t"
               "s_mov_b32 m0, %0"
               : "=&s"(sv) : "v"(v0), "v"(v1), "v"(v2), "v"(v3), "s"(sbase), "s"(m0v) : "memory");
}
DI void glds4_sc1(unsigned v0, unsigned v1, unsigned v2, unsigned v3, const void* sbase, unsigned m0v) {
  unsigned sv;
  asm volatile("s_mov_b32 %0, m0\n\ts_mov_b32 m0, %6\n\ts_nop 0\n\t"
               "global_load_lds_dwordx4 %1, %5 sc1\n\t"
               "global_load_lds_dwordx4 %2, %5 offset:1024 sc1\n\t"
               "global_load_lds_dwordx4 %3, %5 offset:2048 sc1\n\t"
               "global_load_lds_dwordx4 %4, %5 offset:3072 sc1\n\t"
               "s_mov_b32 m0, %0"
               : "=&s"(sv) : "v"(v0), "v"(v1), "v"(v2), "v"(v3), "s"(sbase), "s"(m0v) : "memory");
}
DI void glds1_sc1(unsigned v0, const void* sbase, unsigned m0v) {
  unsigned sv;
  asm volatile("s_mov_b32 %0, m0\n\ts_mov_b32 m0, %3\n\ts_nop 0\n\tglobal_load_lds_dwordx4 %1, %2 sc1\n\ts_mov_b32 m0, %0"
               : "=&s"(sv) : "v"(v0), "s"(sbase), "s"(m0v) : "memory");
}
DI void touch(unsigned voff, const void* sbase, unsigned m0v) {
  unsigned sv;
  asm volatile("s_mov_b32 %0, m0\n\ts_mov_b32 m0, %3\n\ts_nop 0\n\tglobal_load_lds_dword %1, %2\n\ts_mov_b32 m0, %0"
               : "=&s"(sv) : "v"(voff), "s"(sbase), "s"(m0v) : "memory");
}
DI void touch_sc1(unsigned voff, const void* sbase, unsigned m0v) {
  unsigned sv;
  asm volatile("s_mov_b32 %0, m0\n\ts_mov_b32 m0, %3\n\ts_nop 0\n\tglobal_load_lds_dword %1, %2 sc1\n\ts_mov_b32 m0, %0"
               : "=&s"(sv) : "v"(voff), "s"(sbase), "s"(m0v) : "memory");
}
DI void st_gran(char* p, unsigned val, u64 tag) { __hip_atomic_store((gu64*)p, ((u64)(unsigned)tag << 32) | val, RLX_AGENT); }
#define SGB(mask, n) __builtin_amdgcn_sched_group_barrier(mask, n, 0)

constexpr int WR = 65536;
constexpr int GW0 = WR + 57344;
constexpr int SCR = 147456;
constexpr int GWS = WR + 65536;
constexpr int SCR_Q = SCR;
constexpr int SCR_STAT = 159744, SCR_CS = SCR_STAT + 1024, SCR_CG = SCR_STAT + 1536, SCR_TAG = SCR_STAT + 2304, SCR_KN = SCR_STAT + 2320, SCR_M = SCR_STAT + 2560;
constexpr int SCR_DUMMY = 163584;
constexpr int AT_CH = 2 * (int)KVGS;
constexpr int AT_A = 0, AT_B = 98304, AT_C = 32768, AT_H = 121856;
constexpr int AT_RP = 57344;
constexpr int AT_FLAG = 110592, AT_EX = AT_FLAG + 1024;
constexpr int D_STG = 36864;

template <int CTRL> DI float dpp(float x) { return u2f(__builtin_amdgcn_update_dpp(0u, f2u(x), CTRL, 0xf, 0xf, true)); }
DI float sum8(float x) { x += dpp<0xB1>(x); x += dpp<0x4E>(x); x += dpp<0x141>(x); return x; }
DI float sum16(float x) { x = sum8(x); x += dpp<0x140>(x); return x; }
DI float max16(float x) { x = fmaxf(x, dpp<0xB1>(x)); x = fmaxf(x, dpp<0x4E>(x)); x = fmaxf(x, dpp<0x141>(x)); x = fmaxf(x, dpp<0x140>(x)); return x; }
DI float sumx16(float x) { auto r = __builtin_amdgcn_permlane16_swap(f2u(x), f2u(x), false, false); return u2f(r[0]) + u2f(r[1]); }
DI float sumx32(float x) { auto r = __builtin_amdgcn_permlane32_swap(f2u(x), f2u(x), false, false); return adds(u2f(r[0]), u2f(r[1])); }
DI float maxx16(float x) { auto r = __builtin_amdgcn_permlane16_swap(f2u(x), f2u(x), false, false); return fmaxf(u2f(r[0]), u2f(r[1])); }
DI float maxx32(float x) { auto r = __builtin_amdgcn_permlane32_swap(f2u(x), f2u(x), false, false); return fmaxf(u2f(r[0]), u2f(r[1])); }
DI u32x4 bld16_sc1(__amdgpu_buffer_rsrc_t rs, unsigned off) { return __builtin_amdgcn_raw_buffer_load_b128(rs, off, 0, 16); }
DI u32x2 bld8_sc1(__amdgpu_buffer_rsrc_t rs, unsigned off) { return __builtin_amdgcn_raw_buffer_load_b64(rs, off, 0, 16); }
DI float sq2(unsigned u, float c) { const bf16x2_t v = __builtin_bit_cast(bf16x2_t, u); return __builtin_amdgcn_fdot2_f32_bf16(v, v, c, false); }
DI void bar_() { asm volatile("s_barrier" ::: "memory"); }

template <bool SWAP>
DI void qkv_compute(lds_ptr lds, int cw, int lane, int rb, int cb, char* ws TARGS) {
  const int wm = cw >> 1, wn = cw & 1, r = lane & 15, g = lane >> 4;
  lds_ptr xa[2], wa[4];
#pragma unroll
  for (int ks = 0; ks < 2; ++ks) xa[ks] = lds + (wm * 64 + r) * 128 + (((4 * ks + g) ^ ((r >> 1) & 7)) * 16);
  {
    const int q = r >> 2, p = r & 3, f = q | ((g & 1) << 2);
#pragma unroll
    for (int nt = 0; nt < 4; ++nt) wa[nt] = lds + WR + (8 * g + q) * 256 + (((wn * 4 + nt) ^ f) * 32) + p * 8;
  }
  f32x4 acc[4][4];
#pragma unroll
  for (int i = 0; i < 4; ++i)
#pragma unroll
    for (int j = 0; j < 4; ++j) acc[i][j] = f32x4{0.f, 0.f, 0.f, 0.f};
  bf16x8 xf[2][4], wf[2][4];
#define Q_READ(B, SO, KS)                                                                                           \
  do {                                                                                                              \
    _Pragma("unroll") for (int i = 0; i < 4; ++i) {                                                                 \
      wf[B][i] = cat8(ldstr(wa[i] + (SO) + (KS) * 8192), ldstr(wa[i] + (SO) + (KS) * 8192 + 1024));                 \
      xf[B][i] = lds128(xa[KS] + (SO) + i * 2048);                                                                  \
    }                                                                                                               \
  } while (0)
#define Q_MMA(B)                                                                                                    \
  do {                                                                                                              \
    _Pragma("unroll") for (int nt = 0; nt < 4; ++nt)                                                                \
      _Pragma("unroll") for (int mt = 0; mt < 4; ++mt)                                                              \
        acc[nt][mt] = SWAP ? MFMA16(wf[B][nt], xf[B][mt], acc[nt][mt]) : MFMA16(xf[B][mt], wf[B][nt], acc[nt][mt]); \
  } while (0)
#define Q_MIX() do { _Pragma("unroll") for (int i = 0; i < 12; ++i) { SGB(0x8, 1); SGB(0x100, 1); } SGB(0x8, 4); } while (0)
  WVB(1);
  Q_READ(0, 0, 0);
  int so = 0;
#pragma unroll 1
  for (int st = 0; st < 9; ++st) {
    SBAR();
    Q_READ(1, so, 1); Q_MMA(0); Q_MIX();
    SBAR();
    WLB();
    so = (so + 16384) & 65535;
    SBAR();
    Q_READ(0, so, 0); Q_MMA(1); Q_MIX();
  }
  SBAR();
  Q_READ(1, 16384, 1); Q_MMA(0); Q_MIX();
  SBAR();
  Q_MMA(1);
#undef Q_READ
#undef Q_MMA
#undef Q_MIX
  const LAS f32x2* stat = (const LAS f32x2*)(lds + SCR_STAT);
  const LAS float* csum = (const LAS float*)(lds + SCR_CS);
  const int kind = cb / 5, fb = (cb - kind * 5) * 128 + wn * 64;
  if (SWAP) {
    f32x2 st4[4];
    const float qs = kind == 0 ? 0.22360679774997896f * 1.4426950408889634f : 1.0f;
#pragma unroll
    for (int mt = 0; mt < 4; ++mt) { st4[mt] = stat[wm * 64 + mt * 16 + r]; st4[mt][1] *= qs; }
    const char* img = kind == 0 ? ws + WS_Q + (size_t)rb * QGS : ws + WS_KV + (size_t)rb * KVGS;
    const unsigned hs = kind == 0 ? (unsigned)QHS : (unsigned)KVHS;
    const unsigned rowoff = kind == 0 ? r * 16 : ((r >> 3) * 16 + (r & 7)) * 16, mstr = kind == 0 ? 256 : 128;
    float nmax[4]; unsigned kpo[4];
#pragma unroll
    for (int nt = 0; nt < 4; ++nt) {
      const int nl = nt * 16 + 4 * g;
      const f32x4 cs = *(const LAS f32x4*)(csum + wn * 64 + nl);
      const int f0 = fb + nl, head = f0 / 20, sl = (f0 - head * 20) >> 2;
      const unsigned base = head * hs + sl * 1024 + wm * 512 + rowoff;
      nmax[nt] = 0.f; kpo[nt] = (head * 32 + rb * 2 + wm) * 8 + sl;
      unsigned pk[4][2];
#pragma unroll
      for (int mt = 0; mt < 4; ++mt) {
        float v[4];
#pragma unroll
        for (int e = 0; e < 4; ++e) v[e] = (acc[nt][mt][e] - st4[mt][0] * cs[e]) * st4[mt][1];
        pk[mt][0] = cvtpk(v[0], v[1]); pk[mt][1] = cvtpk(v[2], v[3]);
        nmax[nt] = fmaxf(nmax[nt], sq2(pk[mt][1], sq2(pk[mt][0], 0.f)));
      }
      st16_sc1(img, base, u32x4{pk[0][0], pk[0][1], pk[2][0], pk[2][1]});
      st16_sc1(img, base + mstr, u32x4{pk[1][0], pk[1][1], pk[3][0], pk[3][1]});
    }
    if (kind == 1) {
      const bool h0 = r & 1, h1 = r & 2;
#pragma unroll
      for (int nt = 0; nt < 4; ++nt) nmax[nt] = max16(nmax[nt]);
      const float a = h1 ? (h0 ? nmax[3] : nmax[2]) : (h0 ? nmax[1] : nmax[0]);
      const unsigned o = h1 ? (h0 ? kpo[3] : kpo[2]) : (h0 ? kpo[1] : kpo[0]);
      if (r < 4) __hip_atomic_store((GAS float*)(ws + WS_KP) + o, a, RLX_AGENT);
    }
  } else {
    const char* img = ws + WS_KV + (size_t)rb * KVGS + 5120;
    unsigned off[4]; float cs[4];
#pragma unroll
    for (int nt = 0; nt < 4; ++nt) {
      const int nl = nt * 16 + r, f = fb + nl, head = f / 20, d = f - head * 20;
      cs[nt] = csum[wn * 64 + nl];
      off[nt] = head * (unsigned)KVHS + d * 16 + (wm * 4 + (g >> 1)) * 672 + (g & 1) * 336;
    }
#pragma unroll
    for (int j = 0; j < 2; ++j) {
      f32x4 sa[2], sb[2];
#pragma unroll
      for (int i = 0; i < 2; ++i) {
        sa[i] = *(const LAS f32x4*)(stat + wm * 64 + (2 * j + i) * 16 + 4 * g);
        sb[i] = *(const LAS f32x4*)(stat + wm * 64 + (2 * j + i) * 16 + 4 * g + 2);
      }
#pragma unroll
      for (int nt = 0; nt < 4; ++nt) {
        u32x4 o;
#pragma unroll
        for (int i = 0; i < 2; ++i) {
          const f32x4 a = acc[nt][2 * j + i];
          o[2 * i] = cvtpk((a[0] - sa[i][0] * cs[nt]) * sa[i][1], (a[1] - sa[i][2] * cs[nt]) * sa[i][3]);
          o[2 * i + 1] = cvtpk((a[2] - sb[i][0] * cs[nt]) * sb[i][1], (a[3] - sb[i][2] * cs[nt]) * sb[i][3]);
        }
        st16_sc1(img, off[nt] + j * 1344, o);
      }
    }
    {
      const int t = cw * 64 + lane, h0 = ((cb - 10) * 128) / 20, h1 = ((cb - 9) * 128) / 20, hd = h0 + (t >> 4);
      if (hd < h1) st16_sc1(img, hd * (unsigned)KVHS + (t & 15) * 336 + 320, u32x4{0x3f803f80u, 0x3f803f80u, 0x3f803f80u, 0x3f803f80u});
    }
  }
}

DI void gu_compute(lds_ptr lds, int cw, int lane, int rb, int fbk, char* ws TARGS) {
  const int wm = cw >> 1, wn = cw & 1, r = lane & 15, g = lane >> 4;
  lds_ptr xa[2], wa[3];
#pragma unroll
  for (int ks = 0; ks < 2; ++ks) xa[ks] = lds + (wm * 64 + r) * 128 + (((4 * ks + g) ^ ((r >> 1) & 7)) * 16);
#pragma unroll
  for (int nt = 0; nt < 3; ++nt) wa[nt] = lds + (8 * g + (r >> 2)) * 192 + (((wn * 3 + nt) ^ (g & 1)) * 32) + (r & 3) * 8;
  f32x4 acc[6][4];
#pragma unroll
  for (int i = 0; i < 6; ++i)
#pragma unroll
    for (int j = 0; j < 4; ++j) acc[i][j] = f32x4{0.f, 0.f, 0.f, 0.f};
  bf16x8 xf[2][4], wf[2][6];
#define G_READ(B, SO, WO, KS)                                                                                           \
  do {                                                                                                              \
    _Pragma("unroll") for (int i = 0; i < 6; ++i) {                                                                 \
      wf[B][i] = cat8(ldstr(wa[i % 3] + (WO) + (i / 3) * 12288 + (KS) * 6144), ldstr(wa[i % 3] + (WO) + (i / 3) * 12288 + (KS) * 6144 + 768)); \
      if (i < 4) xf[B][i] = lds128(xa[KS] + (SO) + i * 2048);                                                       \
    }                                                                                                               \
  } while (0)
#define G_MMA(B)                                                                                                    \
  do {                                                                                                              \
    _Pragma("unroll") for (int nt = 0; nt < 6; ++nt)                                                                \
      _Pragma("unroll") for (int mt = 0; mt < 4; ++mt) acc[nt][mt] = MFMA16(wf[B][nt], xf[B][mt], acc[nt][mt]);     \
  } while (0)
#define G_MIX() do { _Pragma("unroll") for (int i = 0; i < 16; ++i) { SGB(0x8, 1); SGB(0x100, 1); } SGB(0x8, 8); } while (0)
  int so = 32768, wo = GWS;
  WLB();
  G_READ(0, so, wo, 0);
#pragma unroll 1
  for (int st = 10; st < 19; ++st) {
    SBAR();
    G_READ(1, so, wo, 1); G_MMA(0); G_MIX();
    SBAR();
    if (st == 11) asm volatile("s_waitcnt vmcnt(0)" ::: "memory");
    WLB();
    so = (so + 16384) & 65535;
    wo = wo == GW0 - 49152 ? GW0 : wo == GWS ? GW0 - 24576 : wo - 24576;
    SBAR();
    G_READ(0, so, wo, 0); G_MMA(1); G_MIX();
  }
  SBAR();
  G_READ(1, 49152, GW0, 1); G_MMA(0); G_MIX();
  SBAR();
  WLB();
  G_MMA(1);
#undef G_READ
#undef G_MMA
#undef G_MIX
  const LAS f32x2* stat = (const LAS f32x2*)(lds + SCR_STAT);
  const LAS float* csum = (const LAS float*)(lds + SCR_CG);
  f32x2 st4[4];
#pragma unroll
  for (int mt = 0; mt < 4; ++mt) { st4[mt] = stat[wm * 64 + mt * 16 + r]; st4[mt][1] *= st4[mt][1]; }
  const unsigned ho = (((rb * 8 + wm * 4 + (g & 1)) * 192 + fbk * 12 + wn * 6 + (g >> 1)) * 16 + r) * 16;
#pragma unroll
  for (int nt = 0; nt < 3; ++nt) {
    const f32x4 cg = *(const LAS f32x4*)(csum + wn * 48 + nt * 16 + 4 * g), cu = *(const LAS f32x4*)(csum + 96 + wn * 48 + nt * 16 + 4 * g);
    unsigned w[4][2];
#pragma unroll
    for (int mt = 0; mt < 4; ++mt) {
      float hv[4];
#pragma unroll
      for (int e = 0; e < 4; ++e) {
        const float gv = acc[nt][mt][e] - st4[mt][0] * cg[e], uv = acc[3 + nt][mt][e] - st4[mt][0] * cu[e];
        hv[e] = fmaxf(gv, 0.f) * (uv * st4[mt][1]);
      }
      w[mt][0] = cvtpk(hv[0], hv[1]); w[mt][1] = cvtpk(hv[2], hv[3]);
    }
#pragma unroll
    for (int j = 0; j < 2; ++j) {
      auto s0 = __builtin_amdgcn_permlane16_swap(w[2 * j][0], w[2 * j + 1][0], false, false);
      auto s1 = __builtin_amdgcn_permlane16_swap(w[2 * j][1], w[2 * j + 1][1], false, false);
      st16_sc1(ws + WS_H, ho + j * (2 * 192 * 256) + nt * 512, u32x4{s0[0], s1[0], s0[1], s1[1]});
    }
  }
}

#if defined(__HIP_DEVICE_COMPILE__) && NOPK
#define KATTR __attribute__((target("no-packed-fp32-ops")))
#else
#define KATTR
#endif
__global__ __launch_bounds__(512, 2) KATTR void fused_block(const char* __restrict__ x, const char* __restrict__ wqkv, const char* __restrict__ wo,
                                                      const char* __restrict__ wg, const char* __restrict__ wu, const char* __restrict__ wd,
                                                      char* __restrict__ ws, char* __restrict__ out) {
  __shared__ __attribute__((aligned(16))) char smem[LDS_BYTES];
  lds_ptr lds = (lds_ptr)smem;
  const unsigned ldsb = (unsigned)(size_t)lds;
  const int lane = threadIdx.x & 63, wid = rfl(threadIdx.x >> 6), bid = blockIdx.x;
  const int xcd = bid & 7, idx = bid >> 3;
  const int tq = (xcd & 1) * 30 + idx, cb = tq >> 2, rb = (xcd >> 1) * 4 + (tq & 3);
  const int fbk = (xcd & 1) * 8 + (idx >> 2);
  const int head = xcd * 4 + (idx >> 3), qb = idx & 7;
  const bool has_qkv = idx < 30;
  const int rot_g = ((tq & 3) * 5 + 1) / 2 + (xcd >> 1);
  const int sg0 = rot_g >= 10 ? rot_g - 10 : rot_g, sq0 = (rot_g + cb) % 10;
#define NEXT10(k) ((k) == 9 ? 0 : (k) + 1)
  const int r32 = lane & 31, hh = lane >> 5;
  const char* kvg = ws + WS_KV + (size_t)head * KVHS;
  const unsigned vl = lane * 16;
  u64 tag = 0;
  bf16x8 qa[2]; s16x4 qv[2][2]; float mrow[2];
#define AT_ISSUE(C, SLOT)                                                                                      \
  do {                                                                                                         \
    const char* src_ = kvg + (size_t)(C) * AT_CH;                                                              \
    const unsigned dst_ = ldsb + (SLOT);                                                                       \
    unsigned sv_;                                                                                              \
    asm volatile("s_mov_b32 %0, m0\n\ts_mov_b32 m0, %5\n\ts_nop 0\n\t"                                         \
                 "global_load_lds_dwordx4 %1, %2 sc1\n\tglobal_load_lds_dwordx4 %1, %2 offset:1024 sc1\n\t"    \
                 "global_load_lds_dwordx4 %1, %2 offset:2048 sc1\n\tglobal_load_lds_dwordx4 %1, %2 offset:3072 sc1\n\t" \
                 "s_mov_b32 m0, %6\n\ts_nop 0\n\tglobal_load_lds_dwordx4 %1, %3 sc1\n\t"                       \
                 "s_mov_b32 m0, %7\n\ts_mov_b32 exec_hi, 0\n\tglobal_load_lds_dwordx4 %1, %4 sc1\n\t"          \
                 "s_mov_b32 exec_hi, -1\n\ts_mov_b32 m0, %0"                                                   \
                 : "=&s"(sv_)                                                                                  \
                 : "v"(vl), "s"(src_ + wid * 4096), "s"(src_ + 16384 + wid * 1024), "s"(src_ + 20480), "s"(rfl(dst_ + wid * 4096)), \
                   "s"(rfl(dst_ + 16384 + wid * 1024)), "s"(rfl(dst_ + 20480))                                           \
                 : "memory");                                                                                  \
  } while (0)

#define AT_ISSUE2(C, SLOT)                                                                                     \
  do {                                                                                                         \
    const char* src_ = kvg + (size_t)(C) * AT_CH;                                                              \
    const unsigned dst_ = ldsb + (SLOT);                                                                       \
    glds4_sc1(vl, vl, vl, vl, src_ + wid * 10240, rfl(dst_ + wid * 10240));                                    \
    glds4_sc1(vl, vl, vl, vl, src_ + wid * 10240 + 4096, rfl(dst_ + wid * 10240 + 4096));                      \
    unsigned sv_;                                                                                              \
    asm volatile("s_mov_b32 %0, m0\n\ts_mov_b32 m0, %4\n\ts_nop 0\n\t"                                         \
                 "global_load_lds_dwordx4 %1, %2 sc1\n\tglobal_load_lds_dwordx4 %1, %2 offset:1024 sc1\n\t"    \
                 "s_mov_b32 m0, %5\n\ts_mov_b32 exec_hi, 0\n\tglobal_load_lds_dwordx4 %1, %3 sc1\n\t"          \
                 "s_mov_b32 exec_hi, -1\n\ts_mov_b32 m0, %0"                                                   \
                 : "=&s"(sv_)                                                                                  \
                 : "v"(vl), "s"(src_ + wid * 10240 + 8192), "s"(src_ + 20480), "s"(rfl(dst_ + wid * 10240 + 8192)), "s"(rfl(dst_ + 20480)) \
                 : "memory");                                                                                  \
  } while (0)
  if (wid == 7) {
    const char* pc;
    asm volatile("s_getpc_b64 %0" : "=s"(pc));
    const int ln = idx * ((CODE_LINES + 31) / 32) + (lane < (CODE_LINES + 31) / 32 ? lane : 0);
    touch((ln < CODE_LINES ? ln : 0) * 128, (const char*)((size_t)pc & ~(size_t)255), ldsb + SCR_DUMMY);
  }
  u32x4 rv0, rv1; u32x2 gep;
  asm volatile("global_load_dwordx4 %0, %3, %5\n\tglobal_load_dwordx4 %1, %4, %5 offset:1024\n\tglobal_load_dwordx2 %2, %6, %7 sc1"
               : "=&v"(rv0), "=&v"(rv1), "=&v"(gep) : "v"(vl), "v"(vl & 255), "s"(x + (size_t)(bid * 8 + wid) * 1280), "v"(0u), "s"(ws + WS_EP) : "memory");
  unsigned vx[4], vw[4];
#pragma unroll
  for (int i = 0; i < 4; ++i) {
    const int row = 8 * i + (lane >> 3), ch = (lane & 7) ^ ((4 * i + (lane >> 4)) & 7);
    vx[i] = row * 1280 + ch * 16 - 1024 * i;
    const int k = 4 * i + (lane >> 4), f = (lane >> 4) | ((i >> 1) << 2), cw = (lane & 15) ^ (f << 1);
    vw[i] = k * 3840 + cw * 16 - 1024 * i;
  }
  if (has_qkv) {
    if (wid < 4) {
      const char* p = x + (size_t)(rb * 128 + wid * 32) * 1280;
      glds4(vx[0], vx[1], vx[2], vx[3], p + sq0 * 128, ldsb + wid * 4096);
    } else {
      const char* p = wqkv + cb * 256 + (size_t)(wid - 4) * 16 * 3840;
      glds4(vw[0], vw[1], vw[2], vw[3], p + (size_t)sq0 * (64 * 3840), ldsb + WR + (wid - 4) * 4096);
    }
  }
  {
    if (!has_qkv) asm volatile("s_waitcnt vmcnt(0)" ::: "memory");
    asm volatile("s_waitcnt vmcnt(4)" : "+v"(rv0), "+v"(rv1), "+v"(gep)::"memory");
    tag = make_tag(((u64)gep[1] << 32) | gep[0]);
    float s0 = 0.f, s1 = 0.f;
#pragma unroll
    for (int e = 0; e < 4; ++e) { s0 += bflo(rv0[e]) + bfhi(rv0[e]); s1 += bflo(rv1[e]) + bfhi(rv1[e]); }
    const float mean = sumx32(sumx16(sum16(lane < 16 ? s0 + s1 : s0))) * (1.0f / D);
    float q0 = 0.f, q1 = 0.f;
#pragma unroll
    for (int e = 0; e < 4; ++e) {
      const float a = bflo(rv0[e]) - mean, b = bfhi(rv0[e]) - mean, c = bflo(rv1[e]) - mean, d = bfhi(rv1[e]) - mean;
      q0 += a * a + b * b; q1 += c * c + d * d;
    }
    const float den = sqrtf(bfr(bfr(sumx32(sumx16(sum16(lane < 16 ? q0 + q1 : q0))) * (1.0f / D)) + 1e-5f));
    if (lane == 0) st_gran(ws + WS_ST + (size_t)(bid * 8 + wid) * 8, cvtpk(mean, den), tag);
  }
  if (wid < 2) {
    __builtin_amdgcn_s_setprio(3);
    unsigned vg[6];
#pragma unroll
    for (int j = 0; j < 6; ++j) {
      const int u = j * 64 + lane, k = u / 12, pos = u - k * 12;
      vg[j] = k * 3072 + (pos ^ (((k >> 3) & 1) * 2)) * 16 - 1024 * (j & 3);
    }
    const char* xb = x + (size_t)(rb * 128 + wid * 64) * 1280;
    const unsigned lx = ldsb + wid * 8192;
#define LX_ISSUE(ST, SO) do { glds4(vx[0], vx[1], vx[2], vx[3], xb + (ST) * 128, lx + (SO)); glds4(vx[0], vx[1], vx[2], vx[3], xb + (ST) * 128 + 32 * 1280, lx + 4096 + (SO)); } while (0)
    if (has_qkv) {
      const char* wb = wqkv + cb * 256 + (size_t)wid * 32 * 3840;
#define LQ_X(ST) do { kx = NEXT10(kx); LX_ISSUE(kx, ((ST) & 3) * 16384); } while (0)
#define LQ_W(ST)                                                                                                \
  do {                                                                                                          \
    kw = NEXT10(kw);                                                                                            \
    glds4(vw[0], vw[1], vw[2], vw[3], wb + (size_t)kw * (64 * 3840), lx + WR + ((ST) & 3) * 16384);             \
    glds4(vw[0], vw[1], vw[2], vw[3], wb + (size_t)kw * (64 * 3840) + 16 * 3840, lx + WR + 4096 + ((ST) & 3) * 16384); \
  } while (0)
      int kx = sq0, kw = sq0;
      LQ_X(1); LQ_W(1);
      asm volatile("" : "+v"(vg[0]), "+v"(vg[1]), "+v"(vg[2]), "+v"(vg[3]), "+v"(vg[4]), "+v"(vg[5]));
      WVB(17);
      LQ_X(2); LQ_W(2);
      WVB(16);
      LQ_X(3); LQ_W(3); LQ_X(4);
      WVB(24);
      LQ_W(4); LQ_X(5); LQ_W(5);
#pragma unroll 1
      for (int k = 3; k < 7; ++k) {
        WVB(32);
        LQ_X(k + 3); LQ_W(k + 3);
      }
#undef LQ_X
#undef LQ_W
    }
    const char* wb = (wid ? wu : wg) + fbk * 192;
    const unsigned lw = ldsb + wid * 12288;
#define LG_W(J, WO)                                                                                             \
  do {                                                                                                          \
    glds4(vg[0], vg[1], vg[2], vg[3], wb + (size_t)(J) * (64 * 3072), lw + (WO));                               \
    glds2(vg[4], vg[5], wb + (size_t)(J) * (64 * 3072), lw + 4096 + (WO));                                      \
    glds4(vg[0], vg[1], vg[2], vg[3], wb + (size_t)(J) * (64 * 3072) + 32 * 3072, lw + 6144 + (WO));            \
    glds2(vg[4], vg[5], wb + (size_t)(J) * (64 * 3072) + 32 * 3072, lw + 6144 + 4096 + (WO));                   \
  } while (0)
    int kw = sg0, kx = sg0;
    LG_W(kw, GWS);
    if (has_qkv) { WVB(44); WVB(28); WVB(12); }
    LX_ISSUE(kx, 32768); kx = NEXT10(kx);
    LX_ISSUE(kx, 49152); kx = NEXT10(kx);
    kw = NEXT10(kw); LG_W(kw, GW0 - 24576);
    LX_ISSUE(kx, 0); kx = NEXT10(kx);
    int wo = GW0 - 49152;
#pragma unroll 1
    for (int j = 2; j < 9; ++j) {
      WVB(28);
      kw = NEXT10(kw); LG_W(kw, wo);
      LX_ISSUE(kx, ((j + 3) & 3) * 16384); kx = NEXT10(kx);
      wo = wo == GW0 - 49152 ? GW0 : wo - 24576;
    }
    WVB(28);
    kw = NEXT10(kw); LG_W(kw, GW0);
    {
      const char* qg = ws + WS_Q + (size_t)head * QHS + (size_t)(qb * 2 + wid) * QGS;
      glds4_sc1(vl, vl, vl, vl, qg, ldsb + SCR_Q + wid * 5120);
      glds1_sc1(vl, qg + 4096, ldsb + SCR_Q + wid * 5120 + 4096);
    }
#undef LG_W
#undef LX_ISSUE
    WVB(25);
    AT_ISSUE2(0, AT_A);
    AT_ISSUE2(1, AT_B);
    WVB(22);
  } else if (wid < 4) {
    const int hw = wid - 2, ht = hw * 64 + lane;
    const __amdgpu_buffer_rsrc_t rs = __builtin_amdgcn_make_buffer_rsrc(ws, 0, (int)WS_DBG, 0x00020000);
    __builtin_amdgcn_s_setprio(3);
#define HBAR() do { if (has_qkv) { SBAR(); BAR(); SBAR(); } } while (0)
#define PIN5(V) asm volatile("" : "+v"(V[0]), "+v"(V[1]), "+v"(V[2]), "+v"(V[3]), "+v"(V[4]))
    const bf16x8 ones = {0x3f80, 0x3f80, 0x3f80, 0x3f80, 0x3f80, 0x3f80, 0x3f80, 0x3f80};
    const int r = lane & 15, g = lane >> 4;
    const unsigned t32 = (unsigned)tag;
    char* csd = ws + WS_CS + (size_t)((cb * 16 + sq0) * 128 + hw * 64 + (r >> 2) * 16 + 4 * g + (r & 3)) * 8;
    char* cgd = ws + WS_CG + (size_t)((fbk * 16 + sg0) * 192 + hw * 96 + (r >> 2) * 16 + 4 * g + (r & 3)) * 8;
    unsigned sto = (unsigned)WS_ST + (rb * 128 + ht) * 8;
    unsigned cso = (unsigned)WS_CS + ((has_qkv ? cb : 0) * 16 + hh) * 1024 + hw * 512 + r32 * 16;
    unsigned cgo = (unsigned)WS_CG + fbk * 16 * 1536 + hw * 768 + (lane < 48 ? lane : 0) * 16;
    unsigned kpo = (unsigned)WS_KP + (head * 32 + r32) * 32 + hh * 16;
    const int c0 = (head * 20) >> 7, c1 = (head * 20 + 19) >> 7;
    const char* fkv = ws + WS_FQ + ((5 + (g >> 1) * 5 + ((g & 1) ? c1 : c0)) * 16 + r) * 8;
    const char* fq = ws + WS_FQ + (((lane & 1) ? c1 : c0) * 16 + qb * 2 + hw) * 8;
    lds_ptr wqa[4], wga[2];
#pragma unroll
    for (int nt = 0; nt < 4; ++nt) wqa[nt] = lds + WR + (8 * g + (r >> 2)) * 256 + (((hw * 4 + nt) ^ ((r >> 2) | ((g & 1) << 2))) * 32) + (r & 3) * 8;
#pragma unroll
    for (int i = 0; i < 2; ++i) wga[i] = lds + GWS + hw * 12288 + (8 * g + (r >> 2)) * 192 + ((i ^ (g & 1)) * 32) + (r & 3) * 8;
    asm volatile("" : "+v"(csd), "+v"(cgd), "+v"(sto), "+v"(cso), "+v"(cgo), "+v"(kpo), "+v"(fkv), "+v"(fq), "+v"(wqa[0]), "+v"(wqa[1]), "+v"(wqa[2]), "+v"(wqa[3]),
                 "+v"(wga[0]), "+v"(wga[1]));
    if (has_qkv) {
      SBAR(); WVB(1); SBAR();
      f32x4 cs[4];
#pragma unroll
      for (int nt = 0; nt < 4; ++nt) {
        cs[nt] = MFMA16(cat8(ldstr(wqa[nt]), ldstr(wqa[nt] + 1024)), ones, (f32x4{0.f, 0.f, 0.f, 0.f}));
        cs[nt] = MFMA16(cat8(ldstr(wqa[nt] + 8192), ldstr(wqa[nt] + 9216)), ones, cs[nt]);
      }
      const f32x4 v = (r & 8) ? ((r & 4) ? cs[3] : cs[2]) : ((r & 4) ? cs[1] : cs[0]);
      st_gran(csd, f2u((r & 2) ? ((r & 1) ? v[3] : v[2]) : ((r & 1) ? v[1] : v[0])), tag);
      SBAR(); WLB(); SBAR();
      BAR(); BAR(); BAR(); BAR();
    }
    {
      u32x2 gst = bld8_sc1(rs, sto);
      u32x4 gc[5];
      if (has_qkv) {
#pragma unroll
        for (int i = 0; i < 5; ++i) gc[i] = bld16_sc1(rs, cso + i * 2048);
        BAR(); BAR();
      }
      _Pragma("unroll 1") for (unsigned spins = 0; spins < kSpinCap; ++spins) {
        unsigned bad = gst[1] ^ t32;
        if (has_qkv) {
#pragma unroll
          for (int i = 0; i < 5; ++i) bad |= (gc[i][1] ^ t32) | (gc[i][3] ^ t32);
        }
        if (wall(bad == 0u)) break;
        __builtin_amdgcn_s_sleep(2);
        asm volatile("" ::: "memory");
        gst = bld8_sc1(rs, sto);
        if (has_qkv) {
#pragma unroll
          for (int i = 0; i < 5; ++i) gc[i] = bld16_sc1(rs, cso + i * 2048);
        }
      }
      HBAR();
      *(LAS f32x2*)(lds + SCR_STAT + ht * 8) = f32x2{bflo(gst[0]), 1.0f / bfhi(gst[0])};
      if (has_qkv) {
        float a = 0.f, b = 0.f;
#pragma unroll
        for (int i = 0; i < 5; ++i) { a = adds(a, u2f(gc[i][0])); b = adds(b, u2f(gc[i][2])); }
        a = sumx32(a); b = sumx32(b);
        if (lane < 32) *(LAS f32x2*)(lds + SCR_CS + hw * 256 + lane * 8) = f32x2{a, b};
        WLB();
      }
    }
    BAR();
    {
      f32x4 cs[6];
#pragma unroll
      for (int nt = 0; nt < 6; ++nt) {
        const lds_ptr wa = wga[nt & 1] + (nt >> 1) * 64;
        cs[nt] = MFMA16(cat8(ldstr(wa), ldstr(wa + 768)), ones, (f32x4{0.f, 0.f, 0.f, 0.f}));
        cs[nt] = MFMA16(cat8(ldstr(wa + 6144), ldstr(wa + 6912)), ones, cs[nt]);
      }
      asm volatile("" : "+v"(cs[0]), "+v"(cs[1]), "+v"(cs[2]), "+v"(cs[3]), "+v"(cs[4]), "+v"(cs[5]));
      SBAR(); WLB(); SBAR();
      const f32x4 v = (r & 8) ? ((r & 4) ? cs[3] : cs[2]) : ((r & 4) ? cs[1] : cs[0]), w = (r & 4) ? cs[5] : cs[4];
      st_gran(cgd, f2u((r & 2) ? ((r & 1) ? v[3] : v[2]) : ((r & 1) ? v[1] : v[0])), tag);
      if (r < 8) st_gran(cgd + 512, f2u((r & 2) ? ((r & 1) ? w[3] : w[2]) : ((r & 1) ? w[1] : w[0])), tag);
    }
    BAR();
    if (ht == 0 && has_qkv) set_flag(ws + WS_FQ + (cb * 16 + rb) * 8, tag);
    BAR(); BAR(); BAR();
    {
      const u64 f0 = __hip_atomic_load((gu64*)fkv, RLX_AGENT), f1 = __hip_atomic_load((gu64*)fq, RLX_AGENT);
      BAR();
      if (!wall(f0 == tag && f1 == tag)) wait_tag(fkv, fq, tag);
      __builtin_amdgcn_fence(__ATOMIC_ACQUIRE, "wavefront");
    }
    {
      const u32x4 kp = bld16_sc1(rs, kpo);
      u32x4 gc[10];
#pragma unroll
      for (int i = 0; i < 10; ++i) gc[i] = bld16_sc1(rs, cgo + i * 1536);
      BAR(); BAR();
      const float ks = hh ? u2f(kp[0]) : adds(adds(u2f(kp[0]), u2f(kp[1])), adds(u2f(kp[2]), u2f(kp[3])));
      const float kn2 = maxx16(max16(sumx32(ks)));
      if (ht == 0) *(LAS float*)(lds + SCR_KN) = kn2;
      _Pragma("unroll 1") for (unsigned spins = 0; spins < kSpinCap; ++spins) {
        unsigned bad = 0u;
#pragma unroll
        for (int i = 0; i < 10; ++i) bad |= (gc[i][1] ^ t32) | (gc[i][3] ^ t32);
        if (wall(bad == 0u)) break;
        __builtin_amdgcn_s_sleep(2);
        asm volatile("" ::: "memory");
#pragma unroll
        for (int i = 0; i < 10; ++i) gc[i] = bld16_sc1(rs, cgo + i * 1536);
      }
      SBAR(); WLB(); SBAR();
      float a = 0.f, b = 0.f;
#pragma unroll
      for (int i = 0; i < 10; ++i) { a = adds(a, u2f(gc[i][0])); b = adds(b, u2f(gc[i][2])); }
      if (lane < 48) *(LAS f32x2*)(lds + SCR_CG + hw * 384 + lane * 8) = f32x2{a, b};
    }
  } else {
    const int cw = wid - 4;
    __builtin_amdgcn_s_setprio(2);
    if (has_qkv) {
      if (cb < 10) qkv_compute<true>(lds, cw, lane, rb, cb, ws TPASS);
      else qkv_compute<false>(lds, cw, lane, rb, cb, ws TPASS);
    }
    gu_compute(lds, cw, lane, rb, fbk, ws TPASS);
    __builtin_amdgcn_s_setprio(0);
  }
  {
    const lds_ptr qp = lds + SCR_Q + ((wid >> 1) & 1) * 5120 + (wid & 1) * 512 + r32 * 16;
    const u32x4 l0 = __builtin_bit_cast(u32x4, lds128(qp + hh * 2048)), l1 = __builtin_bit_cast(u32x4, lds128(qp + hh * 2048 + 1024));
    const u32x4 qbb = __builtin_bit_cast(u32x4, lds128(qp + 4096));
#pragma unroll
    for (int a = 0; a < 2; ++a) {
      qa[a] = __builtin_bit_cast(bf16x8, u32x4{l0[2 * a], l0[2 * a + 1], l1[2 * a], l1[2 * a + 1]});
#pragma unroll
      for (int j = 0; j < 2; ++j) { u32x2 u = {hh == j ? qbb[2 * a] : 0u, hh == j ? qbb[2 * a + 1] : 0u}; qv[a][j] = __builtin_bit_cast(s16x4, u); }
    }
    LAS f32x2* mp = (LAS f32x2*)(lds + SCR_M + (wid & 3) * 256 + r32 * 8);
    if (wid < 4) {
      const float kn2 = *(const LAS float*)(lds + SCR_KN);
#pragma unroll
      for (int a = 0; a < 2; ++a) {
        const unsigned w[5] = {hh ? qbb[2 * a + 1] : qbb[2 * a], l0[2 * a], l0[2 * a + 1], l1[2 * a], l1[2 * a + 1]};
        float ss = 0.f;
#pragma unroll
        for (int e = 0; e < 5; ++e) { ss = fmas(bflo(w[e]), bflo(w[e]), ss); ss = fmas(bfhi(w[e]), bfhi(w[e]), ss); }
        mrow[a] = __builtin_amdgcn_sqrtf(fmas(sumx32(ss), kn2 * 1.04f, 0.f));
      }
      *mp = f32x2{mrow[0], mrow[1]};
      WVB(11);
      __builtin_amdgcn_s_setprio(0);
    } else {
      const f32x2 m = *mp;
      mrow[0] = m[0]; mrow[1] = m[1];
    }
  }

  const int kh = wid >> 2;
#define D_ISSUE(ST, XP, XS)                                                                                    \
  do {                                                                                                         \
    const unsigned d_ = rfl(dl0 + ((ST) & 3) * D_STG);                                                         \
    glds4_sc1(vl, vl + (XS) - 1024, vl + 2 * ((XS) - 1024), vl + 3 * ((XS) - 1024), XP, d_);                   \
    glds4_sc1(vl, vl, vl, vl, wpb + (size_t)(ST) * 20480, d_ + 4096);                                          \
    glds1_sc1(vl, wpb + (size_t)(ST) * 20480 + 4096, d_ + 8192);                                               \
  } while (0)
#define D_ISSUE_H(ST) D_ISSUE(ST, hb + (ST) * 4096, 192 * 256)
#define FRESH()                                                                                                \
  int lane_ = lane, bid_ = bid, wq = wid & 3; char* wz = ws;                                                   \
  asm volatile("" : "+v"(lane_), "+s"(bid_), "+s"(wz), "+s"(wq));                                              \
  const int xc = bid_ & 7, ix = bid_ >> 3, mb = (xc >> 1) * 8 + (ix & 7), nb = (xc & 1) * 4 + (ix >> 3);       \
  const unsigned vl = lane_ * 16, dl0 = ldsb + wq * 9216;                                                      \
  const char* wpb = wz + WS_WP + (size_t)(nb * 68 + wq) * 5120;                                                \
  const char* hb = wz + WS_H + (size_t)mb * 4 * 192 * 256 + wq * 1024;                                         \
  (void)vl; (void)dl0; (void)wpb; (void)hb
  {
    const int dsel = r32 <= 20 ? r32 : (r32 < 28 ? 20 : 4);
    const int koff = kh * (int)KVGS + hh * 2048 + r32 * 16, kboff = kh * (int)KVGS + 4096 + lane * 16, voff = kh * (int)KVGS + 5120 + hh * 336 + dsel * 16;
    for (int pass = 0;; ++pass) {
      f32x16 negm[2], sc[2], oacc[2];
#pragma unroll
      for (int a = 0; a < 2; ++a)
#pragma unroll
        for (int e = 0; e < 16; ++e) { negm[a][e] = -mrow[a]; oacc[a][e] = 0.f; }
      asm volatile("" : "+v"(negm[0]), "+v"(negm[1]));
      u32x4 pw[2][2] = {};
      bf16x8 ka, kb, vf[2] = {};
      int sl_cur = AT_A, sl_next = AT_B, sl_free = AT_C;
#define LDKA(P, t) cat8(lds64((P) + ((t) >> 1) * 512 + ((t) & 1) * 8), lds64((P) + 1024 + ((t) >> 1) * 512 + ((t) & 1) * 8))
      ka = LDKA(lds + koff + AT_A, 0);
      kb = lds128(lds + kboff + AT_A);
#define KBH(t) (((t) & 1) ? __builtin_shufflevector(kb, kb, 4, 5, 6, 7) : __builtin_shufflevector(kb, kb, 0, 1, 2, 3))
      sc[0] = MFMA32(ka, qa[0], negm[0]);
      sc[0] = MFMA32K8(KBH(0), qv[0][0], sc[0]);
#define EX4(X, i) do { X[i] = __builtin_amdgcn_exp2f(X[i]); X[i + 1] = __builtin_amdgcn_exp2f(X[i + 1]); X[i + 2] = __builtin_amdgcn_exp2f(X[i + 2]); X[i + 3] = __builtin_amdgcn_exp2f(X[i + 3]); } while (0)
#define PK2(P, j, X, i) do { P[j] = cvtpk(X[i], X[i + 1]); P[j + 1] = cvtpk(X[i + 2], X[i + 3]); } while (0)
#define PFR(a, s) __builtin_bit_cast(bf16x8, pw[a][s])
#define UNIT(A, B, TB, LDK, LDV)                                                             \
  do {                                                                                       \
    sc[B] = MFMA32(ka, qa[B], negm[B]); SBAR();                                              \
    EX4(sc[A], 0); PIN(sc[A]); SBAR();                                                       \
    sc[B] = MFMA32K8(KBH(TB), qv[B][(TB) >> 1], sc[B]); SBAR();                              \
    LDK; SBAR();                                                                             \
    EX4(sc[A], 4); PK2(pw[A][0], 0, sc[A], 0); PIN(sc[A]); PIN(pw[A][0]); SBAR();            \
    oacc[B] = MFMA32(vf[0], PFR(B, 0), oacc[B]); SBAR();                                     \
    EX4(sc[A], 8); PK2(pw[A][0], 2, sc[A], 4); PIN(sc[A]); PIN(pw[A][0]); SBAR();            \
    oacc[B] = MFMA32(vf[1], PFR(B, 1), oacc[B]); SBAR();                                     \
    LDV; SBAR();                                                                             \
    EX4(sc[A], 12); PK2(pw[A][1], 0, sc[A], 8); PK2(pw[A][1], 2, sc[A], 12); PIN(pw[A][1]); SBAR(); \
  } while (0)
#define LDV_(t) do { vf[0] = lds128(vp + (t) * 1344); vf[1] = lds128(vp + (t) * 1344 + 672); } while (0)
      for (int c = 0; c < 8; ++c) {
        lds_ptr kp_ = lds + koff + sl_cur, vp = lds + voff + sl_cur, kn = lds + koff + sl_next, kbn = lds + kboff + sl_next;
        UNIT(0, 1, 0, ka = LDKA(kp_, 1), LDV_(0));
        UNIT(1, 0, 1, (void)0, (void)0);
        UNIT(0, 1, 1, ka = LDKA(kp_, 2), LDV_(1));
        UNIT(1, 0, 2, (void)0, (void)0);
        UNIT(0, 1, 2, ka = LDKA(kp_, 3), LDV_(2));
        UNIT(1, 0, 3, (void)0, (void)0);
        WVB(0);
        if (c == 4 || c == 5) sl_free = AT_H + (c - 4) * AT_CH;
        if (wid < 4) {
          if (c < 6) AT_ISSUE(c + 2, sl_free);
          if (pass == 0 || c == 6) {
            FRESH();
            if (c == 0) {
              if (wq == 0 && lane_ == 0) {
                const int t_ = (xc & 1) * 30 + ix;
                set_flag(wz + WS_FH + (((xc >> 1) * 4 + (t_ & 3)) * 16 + (xc & 1) * 8 + (ix >> 2)) * 8, tag);
              }
              if (bid_ < 136) {
                const int ks = bid_ >> 1;
                unsigned v[5];
#pragma unroll
                for (int j = 0; j < 5; ++j) {
                  const int u = j * 64 + lane_, pr = u / 10, pos = u - pr * 10, k = pr ^ (((pr >> 3) & 1) << 2);
                  const int row = ks < 48 ? k : ks < 64 ? (k >> 4) * 20 + (k & 15) : (k >> 2) * 20 + 16 + (k & 3);
                  v[j] = row * 1280 + pos * 16 - 1024 * (j & 3);
                }
                const char* src = ks < 48 ? wd + (size_t)ks * 32 * 1280 : ks < 64 ? wo + (size_t)(ks - 48) * 40 * 1280 : wo + (size_t)(ks - 64) * 160 * 1280;
                glds5(v, src + ((bid_ & 1) * 4 + wq) * 160, rfl(ldsb + AT_RP + wq * 5120));
              }
            } else if (c == 1) {
              if (bid_ < 136) {
                u32x4 t[5];
#pragma unroll
                for (int j = 0; j < 5; ++j) t[j] = __builtin_bit_cast(u32x4, lds128(lds + AT_RP + wq * 5120 + j * 1024 + vl));
#pragma unroll
                for (int j = 0; j < 5; ++j) st16_sc1(wz + WS_WP + (size_t)(((bid_ & 1) * 4 + wq) * 68 + (bid_ >> 1)) * 5120, vl + j * 1024, t[j]);
              }
            } else if (c == 2) {
              if (bid_ < 136 && wq == 0 && lane_ == 0) set_flag(wz + WS_FW + ((bid_ & 1) * 128 + (bid_ >> 1)) * 8, tag);
            } else if (c == 3) {
              glds1_sc1((unsigned)WS_FH + (xc >> 1) * 512 + (lane_ & 31) * 16, wz, rfl(ldsb + AT_RP + 1024 + wq * 2048));
              glds1_sc1((unsigned)WS_FW + (xc & 1) * 1024 + (lane_ < 34 ? lane_ : 0) * 16, wz, rfl(ldsb + AT_RP + 2048 + wq * 2048));
            } else if (c == 4) {
              const u32x4 f0 = __builtin_bit_cast(u32x4, lds128(lds + AT_RP + 1024 + wq * 2048 + vl));
              const u32x4 f1 = __builtin_bit_cast(u32x4, lds128(lds + AT_RP + 2048 + wq * 2048 + vl));
              const unsigned tl = (unsigned)tag, th = (unsigned)(tag >> 32);
              if (!wall(f0[0] == tl && f0[1] == th && f0[2] == tl && f0[3] == th && f1[0] == tl && f1[1] == th && f1[2] == tl && f1[3] == th)) {
                wait_tag(wz + WS_FH + ((xc >> 1) * 64 + lane_) * 8, wz + WS_FW + ((xc & 1) * 128 + lane_) * 8, tag);
                wait_tag(wz + WS_FW + ((xc & 1) * 128 + 64 + (lane_ & 3)) * 8, tag);
              }
              const unsigned lt = (wq * 64 + lane_) * 128;
              const char* hp = wz + WS_H + (size_t)((xc >> 1) * 32 + ix) * 49152;
              touch_sc1(lt, hp, rfl(ldsb + AT_RP));
              if (wq < 2) touch_sc1(lt, hp + 32768, rfl(ldsb + AT_RP));
            } else if (c == 5) {
              const unsigned lt = (wq * 64 + lane_) * 128;
              const char* wp = wz + WS_WP + (size_t)(xc & 1) * 4 * 68 * 5120 + (size_t)ix * 43520;
              touch_sc1(lt, wp, rfl(ldsb + AT_RP));
              if (wq < 2) touch_sc1(lt < 43520 - 32768 ? lt : 0u, wp + 32768, rfl(ldsb + AT_RP));
            } else if (c == 6) {
              D_ISSUE_H(0); D_ISSUE_H(1); D_ISSUE_H(2);
            }
          }
        }
        SBAR();
        UNIT(0, 1, 3, do { ka = LDKA(kn, 0); kb = lds128(kbn); } while (0), LDV_(3));
        UNIT(1, 0, 0, (void)0, (void)0);
        const int t_ = sl_cur; sl_cur = sl_next; sl_next = sl_free; sl_free = t_;
      }
      oacc[1] = MFMA32(vf[0], PFR(1, 0), oacc[1]);
      oacc[1] = MFMA32(vf[1], PFR(1, 1), oacc[1]);
      {
        LAS f32x4* ex = (LAS f32x4*)(lds + AT_EX + wid * 3072) + lane;
        const f32x16 o = kh ? oacc[0] : oacc[1];
        ex[0] = f32x4{o[0], o[1], o[2], o[3]}; ex[64] = f32x4{o[4], o[5], o[6], o[7]}; ex[128] = f32x4{o[8], o[9], o[10], o[11]};
      }
      WLB();
      const LAS f32x4* in = (const LAS f32x4*)(lds + AT_EX + (wid ^ 4) * 3072) + lane;
      const f32x16 o = kh ? oacc[1] : oacc[0];
      const f32x4 o0 = in[0] + f32x4{o[0], o[1], o[2], o[3]}, o1 = in[64] + f32x4{o[4], o[5], o[6], o[7]}, o2 = in[128] + f32x4{o[8], o[9], o[10], o[11]};
      auto sw = __builtin_amdgcn_permlane32_swap(f2u(o2[0]), f2u(o2[0]), false, false);
      const float ls = u2f(sw[1]);
      const float rl = __builtin_amdgcn_rcpf(ls);
      {
        FRESH();
        const int head = xc * 4 + (ix >> 3), qb = ix & 7, r32 = lane_ & 31, hh = lane_ >> 5;
        const unsigned w0 = cvtpk(o0[0] * rl, o0[1] * rl), w1 = cvtpk(o0[2] * rl, o0[3] * rl), w2 = cvtpk(o1[0] * rl, o1[1] * rl), w3 = cvtpk(o1[2] * rl, o1[3] * rl);
        auto s0 = __builtin_amdgcn_permlane32_swap(w0, w2, false, false);
        auto s1 = __builtin_amdgcn_permlane32_swap(w1, w3, false, false);
        const int tok = qb * 256 + wq * 64 + kh * 32 + r32;
        st16_sc1(wz + WS_O, (((tok >> 4) * 80 + head * 2 + hh) * 16 + (tok & 15)) * 16, u32x4{s0[0], s1[0], s0[1], s1[1]});
        if (hh == 0)
          st8_sc1(wz + WS_O + ((size_t)((tok >> 4) * 80 + 64 + (head >> 1)) * 16 + (tok & 15)) * 16 + (head & 1) * 8,
                  u32x2{cvtpk(o2[0] * rl, o2[1] * rl), cvtpk(o2[2] * rl, o2[3] * rl)});
      }
      const bool anybad = wany(!(ls >= 0x1p-40f && ls <= 0x1p40f));
      if (lane == 0) *(LAS unsigned*)(lds + SCR + 1024 + wid * 4) = (pass == 0 && anybad) ? 1u : 0u;
      WLB();
      const u32x4 fa = *(const LAS u32x4*)(lds + SCR + 1024), fb = *(const LAS u32x4*)(lds + SCR + 1024 + 16);
      if (!rfl(fa[0] | fa[1] | fa[2] | fa[3] | fb[0] | fb[1] | fb[2] | fb[3])) break;
      if (wid < 4) { AT_ISSUE(0, AT_A); AT_ISSUE(1, AT_B); }
      float mx[2] = {-INFINITY, -INFINITY};
      int lane = threadIdx.x & 63;
      PIN(lane);
      const int r32 = lane & 31, hh = lane >> 5;
      for (int gi = 0; gi < 16; ++gi) {
        const char* gp = kvg + (size_t)gi * KVGS;
        kb = __builtin_bit_cast(bf16x8, ld16_sc1_wait(gp + 4096 + lane * 16));
#pragma unroll
        for (int tp = 0; tp < 2; ++tp) {
          const u32x4 k0 = ld16_sc1_wait(gp + hh * 2048 + r32 * 16 + tp * 512), k1 = ld16_sc1_wait(gp + hh * 2048 + r32 * 16 + 1024 + tp * 512);
#pragma unroll
          for (int u = 0; u < 2; ++u) {
            ka = __builtin_bit_cast(bf16x8, u32x4{k0[2 * u], k0[2 * u + 1], k1[2 * u], k1[2 * u + 1]});
#pragma unroll
            for (int a = 0; a < 2; ++a) {
              f32x16 s = MFMA32(ka, qa[a], f32x16{});
              s = MFMA32K8(KBH(u), qv[a][tp], s);
#pragma unroll
              for (int e = 0; e < 16; ++e) mx[a] = fmaxf(mx[a], s[e]);
            }
          }
        }
      }
#pragma unroll
      for (int a = 0; a < 2; ++a) mrow[a] = maxx32(mx[a]);
      WVB(0);
    }
  }
#undef AT_ISSUE
#undef UNIT

  {
  FRESH();
  if (wid < 4) {
    __builtin_amdgcn_s_setprio(3);
    D_ISSUE_H(3);
    const char* ob = wz + WS_O + (size_t)mb * 4 * 80 * 256 + wq * 1024;
    BAR();
    D_ISSUE_H(4);
    WVB(18);
    if (wq == 0 && lane_ == 0) set_flag(wz + WS_FO + ((ix & 7) * 32 + xc * 4 + (ix >> 3)) * 8, tag);
    D_ISSUE_H(5);
#pragma unroll 1
    for (int k = 3; k <= 8; ++k) { WVB(18); D_ISSUE_H(k + 3); }
#pragma unroll 1
    for (int j = 0; j < 5; ++j) { WVB(18); D_ISSUE(12 + j, ob + j * 4096, 80 * 256); }
    WVB(18);
    WVB(9);
    WVB(0);
    if (bid_ == 0 && wq == 0) {
#pragma unroll 1
      for (int i = 0; i < 4; ++i) wait_tag(wz + WS_FH + (i * 64 + lane_) * 8, tag);
      if (lane_ == 0) set_flag(wz + WS_EP, tag);
    }
    BAR();
  } else {
    const int r = lane_ & 15, g = lane_ >> 4;
    lds_ptr xa[4];
#pragma unroll
    for (int i = 0; i < 4; ++i) xa[i] = lds + wq * 9216 + (i ^ wq) * 1024 + vl;
    lds_ptr wa0 = lds + wq * 9216 + 4096 + (8 * g + ((r >> 2) ^ (4 * (g & 1)))) * 160 + (r & 3) * 8;
    lds_ptr wa1 = lds + wq * 9216 + 4096 + (8 * g + ((4 + (r >> 2)) ^ (4 * (g & 1)))) * 160 + (r & 3) * 8;
    f32x4 acc[5][4];
#pragma unroll
    for (int i = 0; i < 5; ++i)
#pragma unroll
      for (int j = 0; j < 4; ++j) acc[i][j] = f32x4{0.f, 0.f, 0.f, 0.f};
    bf16x8 xf[2][4], wf[2][5];
#define D_READ(B, SO)                                                                                  \
  do {                                                                                                 \
    _Pragma("unroll") for (int i = 0; i < 5; ++i) {                                                    \
      wf[B][i] = cat8(ldstr(wa0 + (SO) + i * 32), ldstr(wa1 + (SO) + i * 32));                         \
      if (i < 4) xf[B][i] = lds128(xa[i] + (SO));                                                      \
    }                                                                                                  \
  } while (0)
#define D_MMA(B)                                                                                       \
  do {                                                                                                 \
    _Pragma("unroll") for (int nt = 0; nt < 5; ++nt)                                                   \
      _Pragma("unroll") for (int mt = 0; mt < 4; ++mt) acc[nt][mt] = MFMA16(wf[B][nt], xf[B][mt], acc[nt][mt]); \
  } while (0)
#define D_MIX() do { _Pragma("unroll") for (int i = 0; i < 14; ++i) { SGB(0x8, 1); SGB(0x100, 1); } SGB(0x8, 6); } while (0)
    D_READ(0, 0);
    int so = D_STG;
    const char* fop = wz + WS_FO + (mb >> 2) * 256;
#pragma unroll 1
    for (int st = 0; st < 16; st += 2) {
      if (st == 8) {
        asm volatile("s_waitcnt vmcnt(0)" ::: "memory");
        const u32x4 f = __builtin_bit_cast(u32x4, lds128(lds + SCR + 2048 + wq * 1024 + vl));
        const unsigned tl = (unsigned)tag, th = (unsigned)(tag >> 32);
        if (!wall(f[0] == tl && f[1] == th && f[2] == tl && f[3] == th)) wait_tag(fop + (lane_ & 31) * 8, tag);
      }
      WLB();
      SBAR();
      D_READ(1, so); D_MMA(0); D_MIX();
      so = so == 3 * D_STG ? 0 : so + D_STG;
      SBAR();
      if (st == 0) asm volatile("s_waitcnt vmcnt(0)" ::: "memory");
      if (st == 6) glds1_sc1(r * 16, fop, rfl(ldsb + SCR + 2048 + wq * 1024));
      WLB();
      SBAR();
      D_READ(0, so); D_MMA(1); D_MIX();
      so = so == 3 * D_STG ? 0 : so + D_STG;
      SBAR();
    }
    const unsigned oo = ((mb * 64 + wq * 16 + r) * 640 + nb * 80 + 4 * g) * 2;
    u32x2 xres[5];
#pragma unroll
    for (int nt = 0; nt < 5; ++nt) xres[nt] = *(const u32x2*)(x + oo + nt * 32);
#pragma unroll
    for (int mt = 3; mt >= 0; --mt)
#pragma unroll
      for (int nt = 0; nt < 5; ++nt) {
        acc[nt][mt] = MFMA16(wf[0][nt], xf[0][mt], acc[nt][mt]);
      }
#pragma unroll
    for (int mt = 3; mt >= 1; --mt) {
      LAS f32x4* ex = (LAS f32x4*)(lds + 40960 + ((mt ^ wq) * 3 + mt - 1) * 5120 + vl);
#pragma unroll
      for (int nt = 0; nt < 5; ++nt) ex[nt * 64] = acc[nt][mt];
    }
    WLB();
    const LAS f32x4* in = (const LAS f32x4*)(lds + 40960 + wq * 15360 + vl);
#pragma unroll
    for (int nt = 0; nt < 5; ++nt) {
      const f32x4 a = (acc[nt][0] + in[nt * 64]) + (in[320 + nt * 64] + in[640 + nt * 64]);
      u32x2 o = {cvtpk(bflo(xres[nt][0]) + a[0], bfhi(xres[nt][0]) + a[1]), cvtpk(bflo(xres[nt][1]) + a[2], bfhi(xres[nt][1]) + a[3])};
      *(u32x2*)(out + oo + nt * 32) = o;
    }
  }
  }
}

extern "C" void kernel_launch(void* const* d_in, const int* in_sizes, int n_in, void* d_out, int out_size, void* d_ws, size_t ws_size,
                              hipStream_t stream) {
  fused_block<<<256, 512, 0, stream>>>((const char*)d_in[0], (const char*)d_in[1], (const char*)d_in[2], (const char*)d_in[4],
                                       (const char*)d_in[5], (const char*)d_in[6], (char*)d_ws, (char*)d_out);
}
```

```cpp
#include <hip/hip_runtime.h>
#include <hip/hip_bf16.h>
#include <stdint.h>

#define DI __device__ __forceinline__
#define LAS __attribute__((address_space(3)))
#define GAS __attribute__((address_space(1)))
typedef short bf16x8 __attribute__((ext_vector_type(8)));
typedef short s16x4 __attribute__((ext_vector_type(4)));
typedef float f32x4 __attribute__((ext_vector_type(4)));
typedef float f32x2 __attribute__((ext_vector_type(2)));
typedef float f32x16 __attribute__((ext_vector_type(16)));
typedef unsigned u32x4 __attribute__((ext_vector_type(4)));
typedef unsigned u32x2 __attribute__((ext_vector_type(2)));
typedef __bf16 bf16x2_t __attribute__((ext_vector_type(2)));
typedef LAS char* lds_ptr;
typedef unsigned long long u64;
typedef GAS u64 gu64;

constexpr int S = 2048, D = 640, F = 1536;
constexpr size_t QGS = 5120, QHS = 16 * QGS;
constexpr size_t KVGS = 10496, KVHS = 16 * KVGS;
constexpr size_t WS_EP = 0;
constexpr size_t WS_FQ = 4096;
constexpr size_t WS_FH = WS_FQ + 2048;
constexpr size_t WS_FO = WS_FH + 2048;
constexpr size_t WS_FW = WS_FO + 2048;
constexpr size_t WS_FO4 = 1032192;
constexpr size_t WS_ST = 16384;
constexpr size_t WS_CG = WS_ST + 2048 * 8;
constexpr size_t WS_KP = WS_CG + 16 * 3072 * 8;
constexpr size_t WS_CS = WS_KP + 32 * 32 * 8 * 4;
constexpr size_t WS_Q = 1048576;
constexpr size_t WS_KV = WS_Q + 32 * QHS;
constexpr size_t WS_H = WS_KV + 32 * KVHS + 4096;
constexpr size_t WS_O = WS_H + (size_t)S * F * 2;
constexpr size_t WS_WP = WS_O + (size_t)S * D * 2;
static_assert(WS_CS + 15 * 16 * 128 * 8 <= WS_FO4 && WS_FO4 + 8192 <= WS_Q, "ws map");

#define SBAR() __builtin_amdgcn_sched_barrier(0)
#define PIN(x) asm volatile("" : "+v"(x))
constexpr size_t WS_DBG = (size_t)128 << 20;
#define TS() do {} while (0)
#define TARGS
#define TPASS
#define WVB(N) wait_vm_bar_<N>()
#define WLB() wait_lgkm_bar_()
#define BAR() bar_()
#define MFMA16(a, b, c) __builtin_amdgcn_mfma_f32_16x16x32_bf16(a, b, c, 0, 0, 0)
#define MFMA32(a, b, c) __builtin_amdgcn_mfma_f32_32x32x16_bf16(a, b, c, 0, 0, 0)
#define MFMA32K8(a, b, c) __builtin_amdgcn_mfma_f32_32x32x8bf16_1k(a, b, c, 0, 0, 0)
#define RLX_AGENT __ATOMIC_RELAXED, __HIP_MEMORY_SCOPE_AGENT
#define NOPK 0
#define CODE_LINES 274
#define DOSE 0
constexpr unsigned kSpinCap = 1u << 17;
constexpr int LDS_BYTES = 163840;

DI unsigned f2u(float x) { return __builtin_bit_cast(unsigned, x); }
DI float u2f(unsigned x) { return __builtin_bit_cast(float, x); }
DI bool wall(bool p) { return __builtin_amdgcn_ballot_w64(!p) == 0; }
DI bool wany(bool p) { return __builtin_amdgcn_ballot_w64(p) != 0; }
DI float adds(float a, float b) { float r; asm("v_add_f32 %0, %1, %2" : "=v"(r) : "v"(a), "v"(b)); return r; }
DI float muls(float a, float b) { float r; asm("v_mul_f32 %0, %1, %2" : "=v"(r) : "v"(a), "v"(b)); return r; }
DI float fmas(float a, float b, float c) { float r; asm("v_fma_f32 %0, %1, %2, %3" : "=v"(r) : "v"(a), "v"(b), "v"(c)); return r; }
DI unsigned rfl(unsigned x) { return __builtin_amdgcn_readfirstlane(x); }
DI const char* rflp(const char* p) { const u64 u = (u64)p; return (const char*)(((u64)rfl((unsigned)(u >> 32)) << 32) | rfl((unsigned)u)); }
DI unsigned cvtpk(float lo, float hi) { f32x2 v = {lo, hi}; bf16x2_t b = __builtin_convertvector(v, bf16x2_t); return __builtin_bit_cast(unsigned, b); }
DI float bfr(float x) { return u2f(cvtpk(x, 0.f) << 16); }
DI float bflo(unsigned u) { return u2f(u << 16); }
DI float bfhi(unsigned u) { return u2f(u & 0xffff0000u); }
DI bf16x8 lds128(lds_ptr p) { return *(const LAS bf16x8*)p; }
DI s16x4 lds64(lds_ptr p) { return *(const LAS s16x4*)p; }
DI s16x4 ldstr(lds_ptr p) { return __builtin_amdgcn_ds_read_tr16_b64_v4i16((LAS s16x4*)p); }
DI bf16x8 cat8(s16x4 a, s16x4 b) { return __builtin_shufflevector(a, b, 0, 1, 2, 3, 4, 5, 6, 7); }
template <int N> DI void wait_vm_bar_() { asm volatile("s_waitcnt vmcnt(%0) lgkmcnt(0)\n\ts_barrier" ::"i"(N) : "memory"); }
DI void wait_lgkm_bar_() { asm volatile("s_waitcnt lgkmcnt(0)\n\ts_barrier" ::: "memory"); }
DI void st16_sc1(char* p, u32x4 v) { asm volatile("global_store_dwordx4 %0, %1, off sc1\n\ts_nop 1" ::"v"(p), "v"(v) : "memory"); }
DI void st16_sc1(const char* sb, unsigned off, u32x4 v) { asm volatile("global_store_dwordx4 %0, %1, %2 sc1\n\ts_nop 1" ::"v"(off), "v"(v), "s"(sb) : "memory"); }
DI void st8_sc1(char* p, u32x2 v) { asm volatile("global_store_dwordx2 %0, %1, off sc1\n\ts_nop 1" ::"v"(p), "v"(v) : "memory"); }
DI u32x4 ld16_sc1_wait(const char* p) { u32x4 v; asm volatile("global_load_dwordx4 %0, %1, off sc1\n\ts_waitcnt vmcnt(0)" : "=&v"(v) : "v"(p) : "memory"); return v; }

DI void glds2(unsigned v0, unsigned v1, const void* sbase, unsigned m0v) {
  unsigned sv;
  asm volatile("s_mov_b32 %0, m0\n\ts_mov_b32 m0, %4\n\ts_nop 0\n\t"
               "global_load_lds_dwordx4 %1, %3\n\t"
               "global_load_lds_dwordx4 %2, %3 offset:1024\n\t"
               "s_mov_b32 m0, %0"
               : "=&s"(sv) : "v"(v0), "v"(v1), "s"(sbase), "s"(m0v) : "memory");
}
DI void glds5(const unsigned (&v)[5], const void* sbase, unsigned m0v) {
  unsigned sv;
  asm volatile("s_nop 4\n\ts_mov_b32 %0, m0\n\ts_mov_b32 m0, %7\n\ts_nop 0\n\t"
               "global_load_lds_dwordx4 %1, %6\n\t"
               "global_load_lds_dwordx4 %2, %6 offset:1024\n\t"
               "global_load_lds_dwordx4 %3, %6 offset:2048\n\t"
               "global_load_lds_dwordx4 %4, %6 offset:3072\n\t"
               "s_add_u32 m0, m0, 4096\n\ts_nop 0\n\t"
               "global_load_lds_dwordx4 %5, %6\n\t"
               "s_mov_b32 m0, %0"
               : "=&s"(sv) : "v"(v[0]), "v"(v[1]), "v"(v[2]), "v"(v[3]), "v"(v[4]), "s"(sbase), "s"(m0v) : "memory", "scc");
}
DI void wait_tag(const char* p0, const char* p1, u64 tag) {
  _Pragma("unroll 1") for (unsigned spins = 0; spins < kSpinCap; ++spins) {
    const u64 a = __hip_atomic_load((gu64*)p0, RLX_AGENT), b = __hip_atomic_load((gu64*)p1, RLX_AGENT);
    if (wall(a == tag && b == tag)) break;
    __builtin_amdgcn_s_sleep(2);
  }
  __builtin_amdgcn_fence(__ATOMIC_ACQUIRE, "wavefront");
}
DI void wait_tag(const char* p0, u64 tag) {
  _Pragma("unroll 1") for (unsigned spins = 0; spins < kSpinCap; ++spins) {
    const u64 a = __hip_atomic_load((gu64*)p0, RLX_AGENT);
    if (wall(a == tag)) break;
    __builtin_amdgcn_s_sleep(2);
  }
  __builtin_amdgcn_fence(__ATOMIC_ACQUIRE, "wavefront");
}
DI void set_flag(char* p, u64 tag) { __hip_atomic_store((gu64*)p, tag, RLX_AGENT); }
DI u64 make_tag(u64 e) {
  const u64 u = ((u64)rfl((unsigned)(e >> 32)) << 32) | rfl((unsigned)e);
  return u * 0x9E3779B97F4A7C15ull + 0xD1B54A32D192ED03ull;
}

DI void glds4(unsigned v0, unsigned v1, unsigned v2, unsigned v3, const void* sbase, unsigned m0v) {
  unsigned sv;
  asm volatile("s_mov_b32 %0, m0\n\ts_mov_b32 m0, %6\n\ts_nop 0\n\t"
               "global_load_lds_dwordx4 %1, %5\n\t"
               "global_load_lds_dwordx4 %2, %5 offset:1024\n\t"
               "global_load_lds_dwordx4 %3, %5 offset:2048\n\t"
               "global_load_lds_dwordx4 %4, %5 offset:3072\n\t"
               "s_mov_b32 m0, %0"
               : "=&s"(sv) : "v"(v0), "v"(v1), "v"(v2), "v"(v3), "s"(sbase), "s"(m0v) : "memory");
}
DI void glds4_sc1(unsigned v0, unsigned v1, unsigned v2, unsigned v3, const void* sbase, unsigned m0v) {
  unsigned sv;
  asm volatile("s_mov_b32 %0, m0\n\ts_mov_b32 m0, %6\n\ts_nop 0\n\t"
               "global_load_lds_dwordx4 %1, %5 sc1\n\t"
               "global_load_lds_dwordx4 %2, %5 offset:1024 sc1\n\t"
               "global_load_lds_dwordx4 %3, %5 offset:2048 sc1\n\t"
               "global_load_lds_dwordx4 %4, %5 offset:3072 sc1\n\t"
               "s_mov_b32 m0, %0"
               : "=&s"(sv) : "v"(v0), "v"(v1), "v"(v2), "v"(v3), "s"(sbase), "s"(m0v) : "memory");
}
DI void glds1_sc1(unsigned v0, const void* sbase, unsigned m0v) {
  unsigned sv;
  asm volatile("s_mov_b32 %0, m0\n\ts_mov_b32 m0, %3\n\ts_nop 0\n\tglobal_load_lds_dwordx4 %1, %2 sc1\n\ts_mov_b32 m0, %0"
               : "=&s"(sv) : "v"(v0), "s"(sbase), "s"(m0v) : "memory");
}
DI void touch(unsigned voff, const void* sbase, unsigned m0v) {
  unsigned sv;
  asm volatile("s_mov_b32 %0, m0\n\ts_mov_b32 m0, %3\n\ts_nop 0\n\tglobal_load_lds_dword %1, %2\n\ts_mov_b32 m0, %0"
               : "=&s"(sv) : "v"(voff), "s"(sbase), "s"(m0v) : "memory");
}
DI void touch_sc1(unsigned voff, const void* sbase, unsigned m0v) {
  unsigned sv;
  asm volatile("s_mov_b32 %0, m0\n\ts_mov_b32 m0, %3\n\ts_nop 0\n\tglobal_load_lds_dword %1, %2 sc1\n\ts_mov_b32 m0, %0"
               : "=&s"(sv) : "v"(voff), "s"(sbase), "s"(m0v) : "memory");
}
DI void st_gran(char* p, unsigned val, u64 tag) { __hip_atomic_store((gu64*)p, ((u64)(unsigned)tag << 32) | val, RLX_AGENT); }
#define SGB(mask, n) __builtin_amdgcn_sched_group_barrier(mask, n, 0)

constexpr int WR = 65536;
constexpr int GW0 = WR + 57344;
constexpr int SCR = 147456;
constexpr int GWS = WR + 65536;
constexpr int SCR_Q = SCR;
constexpr int SCR_STAT = 159744, SCR_CS = SCR_STAT + 1024, SCR_CG = SCR_STAT + 1536, SCR_TAG = SCR_STAT + 2304, SCR_KN = SCR_STAT + 2320, SCR_M = SCR_STAT + 2560;
constexpr int SCR_DUMMY = 163584;
constexpr int AT_CH = 2 * (int)KVGS;
constexpr int AT_A = 0, AT_B = 98304;
constexpr int AT_6 = 142848, AT_7 = 121088;
constexpr int AT_RP = AT_7;
constexpr int AT_FL = 83968;
constexpr int AT_BAD = 147456;
constexpr int D_STG = 36864;

template <int CTRL> DI float dpp(float x) { return u2f(__builtin_amdgcn_update_dpp(0u, f2u(x), CTRL, 0xf, 0xf, true)); }
DI float sum8(float x) { x += dpp<0xB1>(x); x += dpp<0x4E>(x); x += dpp<0x141>(x); return x; }
DI float sum16(float x) { x = sum8(x); x += dpp<0x140>(x); return x; }
DI float max16(float x) { x = fmaxf(x, dpp<0xB1>(x)); x = fmaxf(x, dpp<0x4E>(x)); x = fmaxf(x, dpp<0x141>(x)); x = fmaxf(x, dpp<0x140>(x)); return x; }
template <int CTRL> DI unsigned dppu(unsigned x) { return __builtin_amdgcn_update_dpp(0u, x, CTRL, 0xf, 0xf, true); }
DI unsigned umax16(unsigned x) { x = max(x, dppu<0xB1>(x)); x = max(x, dppu<0x4E>(x)); x = max(x, dppu<0x141>(x)); x = max(x, dppu<0x140>(x)); return x; }
DI float sumx16(float x) { auto r = __builtin_amdgcn_permlane16_swap(f2u(x), f2u(x), false, false); return u2f(r[0]) + u2f(r[1]); }
DI float sumx32(float x) { auto r = __builtin_amdgcn_permlane32_swap(f2u(x), f2u(x), false, false); return adds(u2f(r[0]), u2f(r[1])); }
DI float maxx16(float x) { auto r = __builtin_amdgcn_permlane16_swap(f2u(x), f2u(x), false, false); return fmaxf(u2f(r[0]), u2f(r[1])); }
DI float maxx32(float x) { auto r = __builtin_amdgcn_permlane32_swap(f2u(x), f2u(x), false, false); return fmaxf(u2f(r[0]), u2f(r[1])); }
DI u32x4 bld16_sc1(__amdgpu_buffer_rsrc_t rs, unsigned off) { return __builtin_amdgcn_raw_buffer_load_b128(rs, off, 0, 16); }
DI u32x2 bld8_sc1(__amdgpu_buffer_rsrc_t rs, unsigned off) { return __builtin_amdgcn_raw_buffer_load_b64(rs, off, 0, 16); }
DI float sq2(unsigned u, float c) { const bf16x2_t v = __builtin_bit_cast(bf16x2_t, u); return __builtin_amdgcn_fdot2_f32_bf16(v, v, c, false); }
DI void bar_() { asm volatile("s_barrier" ::: "memory"); }

struct Pend { u32x4 v[4][2]; unsigned off[4]; const char* base; unsigned str; };
template <bool SWAP>
DI void qkv_compute(lds_ptr lds, int cw, int lane, int rb, int cb, char* ws, Pend& pd TARGS) {
  const int wm = cw >> 1, wn = cw & 1, r = lane & 15, g = lane >> 4;
  lds_ptr xa[2], wa[4];
#pragma unroll
  for (int ks = 0; ks < 2; ++ks) xa[ks] = lds + (wm * 64 + r) * 128 + (((4 * ks + g) ^ ((r >> 1) & 7)) * 16);
  {
    const int q = r >> 2, p = r & 3, f = q | ((g & 1) << 2);
#pragma unroll
    for (int nt = 0; nt < 4; ++nt) wa[nt] = lds + WR + (8 * g + q) * 256 + (((wn * 4 + nt) ^ f) * 32) + p * 8;
  }
  f32x4 acc[4][4];
#pragma unroll
  for (int i = 0; i < 4; ++i)
#pragma unroll
    for (int j = 0; j < 4; ++j) acc[i][j] = f32x4{0.f, 0.f, 0.f, 0.f};
  bf16x8 xf[2][4], wf[2][4];
#define Q_READ(B, SO, KS)                                                                                           \
  do {                                                                                                              \
    _Pragma("unroll") for (int i = 0; i < 4; ++i) {                                                                 \
      wf[B][i] = cat8(ldstr(wa[i] + (SO) + (KS) * 8192), ldstr(wa[i] + (SO) + (KS) * 8192 + 1024));                 \
      xf[B][i] = lds128(xa[KS] + (SO) + i * 2048);                                                                  \
    }                                                                                                               \
  } while (0)
#define Q_MMA(B)                                                                                                    \
  do {                                                                                                              \
    _Pragma("unroll") for (int nt = 0; nt < 4; ++nt)                                                                \
      _Pragma("unroll") for (int mt = 0; mt < 4; ++mt)                                                              \
        acc[nt][mt] = SWAP ? MFMA16(wf[B][nt], xf[B][mt], acc[nt][mt]) : MFMA16(xf[B][mt], wf[B][nt], acc[nt][mt]); \
  } while (0)
#define Q_MIX() do { _Pragma("unroll") for (int i = 0; i < 12; ++i) { SGB(0x8, 1); SGB(0x100, 1); } SGB(0x8, 4); } while (0)
  WVB(1);
  Q_READ(0, 0, 0);
  int so = 0;
#pragma unroll 1
  for (int st = 0; st < 9; ++st) {
    SBAR();
    Q_READ(1, so, 1); Q_MMA(0); Q_MIX();
    SBAR();
    WLB();
    so = (so + 16384) & 65535;
    SBAR();
    Q_READ(0, so, 0); Q_MMA(1); Q_MIX();
  }
  SBAR();
  Q_READ(1, 16384, 1); Q_MMA(0); Q_MIX();
  SBAR();
  Q_MMA(1);
#undef Q_READ
#undef Q_MMA
#undef Q_MIX
  const LAS f32x2* stat = (const LAS f32x2*)(lds + SCR_STAT);
  const LAS float* csum = (const LAS float*)(lds + SCR_CS);
  const int kind = cb / 5, fb = (cb - kind * 5) * 128 + wn * 64;
  if (SWAP) {
    f32x2 st4[4];
    const float qs = kind == 0 ? 0.22360679774997896f * 1.4426950408889634f : 1.0f;
#pragma unroll
    for (int mt = 0; mt < 4; ++mt) { st4[mt] = stat[wm * 64 + mt * 16 + r]; st4[mt][1] *= qs; }
    const char* img = kind == 0 ? ws + WS_Q + (size_t)rb * QGS : ws + WS_KV + (size_t)rb * KVGS;
    const unsigned hs = kind == 0 ? (unsigned)QHS : (unsigned)KVHS;
    const unsigned rowoff = kind == 0 ? r * 16 : ((r >> 3) * 16 + (r & 7)) * 16, mstr = kind == 0 ? 256 : 128;
    float nmax[4]; unsigned kpo[4];
#pragma unroll
    for (int nt = 0; nt < 4; ++nt) {
      const int nl = nt * 16 + 4 * g;
      const f32x4 cs = *(const LAS f32x4*)(csum + wn * 64 + nl);
      const int f0 = fb + nl, head = f0 / 20, sl = (f0 - head * 20) >> 2;
      const unsigned base = head * hs + sl * 1024 + wm * 512 + rowoff;
      nmax[nt] = 0.f; kpo[nt] = (head * 32 + rb * 2 + wm) * 8 + sl;
      unsigned pk[4][2];
#pragma unroll
      for (int mt = 0; mt < 4; ++mt) {
        float v[4];
#pragma unroll
        for (int e = 0; e < 4; ++e) v[e] = (acc[nt][mt][e] - st4[mt][0] * cs[e]) * st4[mt][1];
        pk[mt][0] = cvtpk(v[0], v[1]); pk[mt][1] = cvtpk(v[2], v[3]);
        nmax[nt] = fmaxf(nmax[nt], sq2(pk[mt][1], sq2(pk[mt][0], 0.f)));
      }
      pd.off[nt] = base;
      pd.v[nt][0] = u32x4{pk[0][0], pk[0][1], pk[2][0], pk[2][1]};
      pd.v[nt][1] = u32x4{pk[1][0], pk[1][1], pk[3][0], pk[3][1]};
    }
    pd.base = img; pd.str = mstr;
    if (kind == 1) {
      const bool h0 = r & 1, h1 = r & 2;
#pragma unroll
      for (int nt = 0; nt < 4; ++nt) nmax[nt] = u2f(umax16(f2u(nmax[nt])));
      const float a = h1 ? (h0 ? nmax[3] : nmax[2]) : (h0 ? nmax[1] : nmax[0]);
      const unsigned o = h1 ? (h0 ? kpo[3] : kpo[2]) : (h0 ? kpo[1] : kpo[0]);
      if (r < 4) __hip_atomic_store((GAS float*)(ws + WS_KP) + o, a, RLX_AGENT);
    }
  } else {
    const char* img = ws + WS_KV + (size_t)rb * KVGS + 5120;
    unsigned off[4]; float cs[4];
#pragma unroll
    for (int nt = 0; nt < 4; ++nt) {
      const int nl = nt * 16 + r, f = fb + nl, head = f / 20, d = f - head * 20;
      cs[nt] = csum[wn * 64 + nl];
      off[nt] = head * (unsigned)KVHS + d * 16 + (wm * 4 + (g >> 1)) * 672 + (g & 1) * 336;
    }
#pragma unroll
    for (int j = 0; j < 2; ++j) {
      f32x4 sa[2], sb[2];
#pragma unroll
      for (int i = 0; i < 2; ++i) {
        sa[i] = *(const LAS f32x4*)(stat + wm * 64 + (2 * j + i) * 16 + 4 * g);
        sb[i] = *(const LAS f32x4*)(stat + wm * 64 + (2 * j + i) * 16 + 4 * g + 2);
      }
#pragma unroll
      for (int nt = 0; nt < 4; ++nt) {
        u32x4 o;
#pragma unroll
        for (int i = 0; i < 2; ++i) {
          const f32x4 a = acc[nt][2 * j + i];
          o[2 * i] = cvtpk((a[0] - sa[i][0] * cs[nt]) * sa[i][1], (a[1] - sa[i][2] * cs[nt]) * sa[i][3]);
          o[2 * i + 1] = cvtpk((a[2] - sb[i][0] * cs[nt]) * sb[i][1], (a[3] - sb[i][2] * cs[nt]) * sb[i][3]);
        }
        pd.v[nt][j] = o;
      }
    }
#pragma unroll
    for (int nt = 0; nt < 4; ++nt) pd.off[nt] = off[nt];
    pd.base = img; pd.str = 1344;
    {
      const int t = cw * 64 + lane, h0 = ((cb - 10) * 128) / 20, h1 = ((cb - 9) * 128) / 20, hd = h0 + (t >> 4);
      if (hd < h1) st16_sc1(img, hd * (unsigned)KVHS + (t & 15) * 336 + 320, u32x4{0x3f803f80u, 0x3f803f80u, 0x3f803f80u, 0x3f803f80u});
    }
  }
}

DI void gu_compute(lds_ptr lds, int cw, int lane, int rb, int fbk, char* ws, const Pend& pd, bool has_pd TARGS) {
  const int wm = cw >> 1, wn = cw & 1, r = lane & 15, g = lane >> 4;
  lds_ptr xa[2], wa[3];
#pragma unroll
  for (int ks = 0; ks < 2; ++ks) xa[ks] = lds + (wm * 64 + r) * 128 + (((4 * ks + g) ^ ((r >> 1) & 7)) * 16);
#pragma unroll
  for (int nt = 0; nt < 3; ++nt) wa[nt] = lds + (8 * g + (r >> 2)) * 192 + (((wn * 3 + nt) ^ (g & 1)) * 32) + (r & 3) * 8;
  f32x4 acc[6][4];
#pragma unroll
  for (int i = 0; i < 6; ++i)
#pragma unroll
    for (int j = 0; j < 4; ++j) acc[i][j] = f32x4{0.f, 0.f, 0.f, 0.f};
  bf16x8 xf[2][4], wf[2][6];
#define G_READ(B, SO, WO, KS)                                                                                           \
  do {                                                                                                              \
    _Pragma("unroll") for (int i = 0; i < 6; ++i) {                                                                 \
      wf[B][i] = cat8(ldstr(wa[i % 3] + (WO) + (i / 3) * 12288 + (KS) * 6144), ldstr(wa[i % 3] + (WO) + (i / 3) * 12288 + (KS) * 6144 + 768)); \
      if (i < 4) xf[B][i] = lds128(xa[KS] + (SO) + i * 2048);                                                       \
    }                                                                                                               \
  } while (0)
#define G_MMA(B)                                                                                                    \
  do {                                                                                                              \
    _Pragma("unroll") for (int nt = 0; nt < 6; ++nt)                                                                \
      _Pragma("unroll") for (int mt = 0; mt < 4; ++mt) acc[nt][mt] = MFMA16(wf[B][nt], xf[B][mt], acc[nt][mt]);     \
  } while (0)
#define G_MIX() do { _Pragma("unroll") for (int i = 0; i < 16; ++i) { SGB(0x8, 1); SGB(0x100, 1); } SGB(0x8, 8); } while (0)
  int so = 32768, wo = GWS;
  if (has_pd) {
#pragma unroll
    for (int i = 0; i < 4; ++i) { st16_sc1(pd.base, pd.off[i], pd.v[i][0]); st16_sc1(pd.base + pd.str, pd.off[i], pd.v[i][1]); }
  }
  WLB();
  G_READ(0, so, wo, 0);
#pragma unroll 1
  for (int st = 10; st < 19; ++st) {
    SBAR();
    G_READ(1, so, wo, 1); G_MMA(0); G_MIX();
    SBAR();
    if (st == 11) asm volatile("s_waitcnt vmcnt(0)" ::: "memory");
    WLB();
    so = (so + 16384) & 65535;
    wo = wo == GW0 - 49152 ? GW0 : wo == GWS ? GW0 - 24576 : wo - 24576;
    SBAR();
    G_READ(0, so, wo, 0); G_MMA(1); G_MIX();
  }
  SBAR();
  G_READ(1, 49152, GW0, 1); G_MMA(0); G_MIX();
  SBAR();
  WLB();
  G_MMA(1);
#undef G_READ
#undef G_MMA
#undef G_MIX
  const LAS f32x2* stat = (const LAS f32x2*)(lds + SCR_STAT);
  const LAS float* csum = (const LAS float*)(lds + SCR_CG);
  f32x2 st4[4];
#pragma unroll
  for (int mt = 0; mt < 4; ++mt) { st4[mt] = stat[wm * 64 + mt * 16 + r]; st4[mt][1] *= st4[mt][1]; }
  const unsigned ho = (((rb * 8 + wm * 4 + (g & 1)) * 192 + fbk * 12 + wn * 6 + (g >> 1)) * 16 + r) * 16;
#pragma unroll
  for (int nt = 0; nt < 3; ++nt) {
    const f32x4 cg = *(const LAS f32x4*)(csum + wn * 48 + nt * 16 + 4 * g), cu = *(const LAS f32x4*)(csum + 96 + wn * 48 + nt * 16 + 4 * g);
    unsigned w[4][2];
#pragma unroll
    for (int mt = 0; mt < 4; ++mt) {
      float hv[4];
#pragma unroll
      for (int e = 0; e < 4; ++e) {
        const float gv = acc[nt][mt][e] - st4[mt][0] * cg[e], uv = acc[3 + nt][mt][e] - st4[mt][0] * cu[e];
        hv[e] = fmaxf(gv, 0.f) * (uv * st4[mt][1]);
      }
      w[mt][0] = cvtpk(hv[0], hv[1]); w[mt][1] = cvtpk(hv[2], hv[3]);
    }
#pragma unroll
    for (int j = 0; j < 2; ++j) {
      auto s0 = __builtin_amdgcn_permlane16_swap(w[2 * j][0], w[2 * j + 1][0], false, false);
      auto s1 = __builtin_amdgcn_permlane16_swap(w[2 * j][1], w[2 * j + 1][1], false, false);
      st16_sc1(ws + WS_H, ho + j * (2 * 192 * 256) + nt * 512, u32x4{s0[0], s1[0], s0[1], s1[1]});
    }
  }
}

#if defined(__HIP_DEVICE_COMPILE__) && NOPK
#define KATTR __attribute__((target("no-packed-fp32-ops")))
#else
#define KATTR
#endif
__global__ __launch_bounds__(512, 2) KATTR void fused_block(const char* __restrict__ x, const char* __restrict__ wqkv, const char* __restrict__ wo,
                                                      const char* __restrict__ wg, const char* __restrict__ wu, const char* __restrict__ wd,
                                                      char* __restrict__ ws, char* __restrict__ out) {
  __shared__ __attribute__((aligned(16))) char smem[LDS_BYTES];
  lds_ptr lds = (lds_ptr)smem;
  const unsigned ldsb = (unsigned)(size_t)lds;
  const int lane = threadIdx.x & 63, wid = rfl(threadIdx.x >> 6), bid = blockIdx.x;
  const int xcd = bid & 7, idx = bid >> 3;
  const int tq = (xcd & 1) * 30 + idx, cb = tq >> 2, rb = (xcd >> 1) * 4 + (tq & 3);
  const int fbk = (xcd & 1) * 8 + (idx >> 2);
  const int head = xcd * 4 + (idx >> 3), qb = idx & 7;
  const bool has_qkv = idx < 30;
  const int rot_g = ((tq & 3) * 5 + 1) / 2 + (xcd >> 1);
  const int sg0 = rot_g >= 10 ? rot_g - 10 : rot_g, sq0 = (rot_g + cb) % 10;
#define NEXT10(k) ((k) == 9 ? 0 : (k) + 1)
  const int r32 = lane & 31, hh = lane >> 5;
  const char* kvg = ws + WS_KV + (size_t)head * KVHS;
  const unsigned vl = lane * 16;
  u64 tag = 0;
  bf16x8 qa[2]; s16x4 qv[2][2]; float mrow[2];
#define AT_ISSUE(C, SLOT)                                                                                      \
  do {                                                                                                         \
    const char* src_ = kvg + (size_t)(C) * AT_CH;                                                              \
    const unsigned dst_ = ldsb + (SLOT);                                                                       \
    unsigned sv_;                                                                                              \
    asm volatile("s_mov_b32 %0, m0\n\ts_mov_b32 m0, %5\n\ts_nop 0\n\t"                                         \
                 "global_load_lds_dwordx4 %1, %2 sc1\n\tglobal_load_lds_dwordx4 %1, %2 offset:1024 sc1\n\t"    \
                 "global_load_lds_dwordx4 %1, %2 offset:2048 sc1\n\tglobal_load_lds_dwordx4 %1, %2 offset:3072 sc1\n\t" \
                 "s_mov_b32 m0, %6\n\ts_nop 0\n\tglobal_load_lds_dwordx4 %1, %3 sc1\n\t"                       \
                 "s_mov_b32 m0, %7\n\ts_mov_b32 exec_hi, 0\n\tglobal_load_lds_dwordx4 %1, %4 sc1\n\t"          \
                 "s_mov_b32 exec_hi, -1\n\ts_mov_b32 m0, %0"                                                   \
                 : "=&s"(sv_)                                                                                  \
                 : "v"(vl), "s"(src_ + wq * 4096), "s"(src_ + 16384 + wq * 1024), "s"(src_ + 20480), "s"(rfl(dst_ + wq * 4096)), \
                   "s"(rfl(dst_ + 16384 + wq * 1024)), "s"(rfl(dst_ + 20480))                                           \
                 : "memory");                                                                                  \
  } while (0)

#define AT_ISSUE2(C, SLOT)                                                                                     \
  do {                                                                                                         \
    const char* src_ = kvg + (size_t)(C) * AT_CH;                                                              \
    const unsigned dst_ = ldsb + (SLOT);                                                                       \
    glds4_sc1(vl, vl, vl, vl, src_ + wid * 10240, rfl(dst_ + wid * 10240));                                    \
    glds4_sc1(vl, vl, vl, vl, src_ + wid * 10240 + 4096, rfl(dst_ + wid * 10240 + 4096));                      \
    unsigned sv_;                                                                                              \
    asm volatile("s_mov_b32 %0, m0\n\ts_mov_b32 m0, %4\n\ts_nop 0\n\t"                                         \
                 "global_load_lds_dwordx4 %1, %2 sc1\n\tglobal_load_lds_dwordx4 %1, %2 offset:1024 sc1\n\t"    \
                 "s_mov_b32 m0, %5\n\ts_mov_b32 exec_hi, 0\n\tglobal_load_lds_dwordx4 %1, %3 sc1\n\t"          \
                 "s_mov_b32 exec_hi, -1\n\ts_mov_b32 m0, %0"                                                   \
                 : "=&s"(sv_)                                                                                  \
                 : "v"(vl), "s"(src_ + wid * 10240 + 8192), "s"(src_ + 20480), "s"(rfl(dst_ + wid * 10240 + 8192)), "s"(rfl(dst_ + 20480)) \
                 : "memory");                                                                                  \
  } while (0)
  if (wid == 7) {
    const char* pc;
    asm volatile("s_getpc_b64 %0" : "=s"(pc));
    const int ln = idx * ((CODE_LINES + 31) / 32) + (lane < (CODE_LINES + 31) / 32 ? lane : 0);
    touch((ln < CODE_LINES ? ln : 0) * 128, (const char*)((size_t)pc & ~(size_t)255), ldsb + SCR_DUMMY);
  }
  u32x4 rv0, rv1; u32x2 gep;
  asm volatile("global_load_dwordx4 %0, %3, %5\n\tglobal_load_dwordx4 %1, %4, %5 offset:1024\n\tglobal_load_dwordx2 %2, %6, %7 sc1"
               : "=&v"(rv0), "=&v"(rv1), "=&v"(gep) : "v"(vl), "v"(vl & 255), "s"(x + (size_t)(bid * 8 + wid) * 1280), "v"(0u), "s"(ws + WS_EP) : "memory");
  unsigned vx[4], vw[4];
#pragma unroll
  for (int i = 0; i < 4; ++i) {
    const int row = 8 * i + (lane >> 3), ch = (lane & 7) ^ ((4 * i + (lane >> 4)) & 7);
    vx[i] = row * 1280 + ch * 16 - 1024 * i;
    const int k = 4 * i + (lane >> 4), f = (lane >> 4) | ((i >> 1) << 2), cw = (lane & 15) ^ (f << 1);
    vw[i] = k * 3840 + cw * 16 - 1024 * i;
  }
  if (has_qkv) {
    if (wid < 4) {
      const char* p = x + (size_t)(rb * 128 + wid * 32) * 1280;
      glds4(vx[0], vx[1], vx[2], vx[3], p + sq0 * 128, ldsb + wid * 4096);
    } else {
      const char* p = wqkv + cb * 256 + (size_t)(wid - 4) * 16 * 3840;
      glds4(vw[0], vw[1], vw[2], vw[3], p + (size_t)sq0 * (64 * 3840), ldsb + WR + (wid - 4) * 4096);
    }
  }
  {
    if (!has_qkv) asm volatile("s_waitcnt vmcnt(0)" ::: "memory");
    asm volatile("s_waitcnt vmcnt(4)" : "+v"(rv0), "+v"(rv1), "+v"(gep)::"memory");
    tag = make_tag(((u64)gep[1] << 32) | gep[0]);
    float s0 = 0.f, s1 = 0.f;
#pragma unroll
    for (int e = 0; e < 4; ++e) { s0 += bflo(rv0[e]) + bfhi(rv0[e]); s1 += bflo(rv1[e]) + bfhi(rv1[e]); }
    const float mean = sumx32(sumx16(sum16(lane < 16 ? s0 + s1 : s0))) * (1.0f / D);
    float q0 = 0.f, q1 = 0.f;
#pragma unroll
    for (int e = 0; e < 4; ++e) {
      const float a = bflo(rv0[e]) - mean, b = bfhi(rv0[e]) - mean, c = bflo(rv1[e]) - mean, d = bfhi(rv1[e]) - mean;
      q0 += a * a + b * b; q1 += c * c + d * d;
    }
    const float den = sqrtf(bfr(bfr(sumx32(sumx16(sum16(lane < 16 ? q0 + q1 : q0))) * (1.0f / D)) + 1e-5f));
    if (lane == 0) st_gran(ws + WS_ST + (size_t)(bid * 8 + wid) * 8, cvtpk(mean, den), tag);
  }
  if (wid < 2) {
    __builtin_amdgcn_s_setprio(3);
    unsigned vg[6];
#pragma unroll
    for (int j = 0; j < 6; ++j) {
      const int u = j * 64 + lane, k = u / 12, pos = u - k * 12;
      vg[j] = k * 3072 + (pos ^ (((k >> 3) & 1) * 2)) * 16 - 1024 * (j & 3);
    }
    const char* xb = x + (size_t)(rb * 128 + wid * 64) * 1280;
    const unsigned lx = ldsb + wid * 8192;
#define LX_ISSUE(ST, SO) do { glds4(vx[0], vx[1], vx[2], vx[3], xb + (ST) * 128, lx + (SO)); glds4(vx[0], vx[1], vx[2], vx[3], xb + (ST) * 128 + 32 * 1280, lx + 4096 + (SO)); } while (0)
    if (has_qkv) {
      const char* wb = wqkv + cb * 256 + (size_t)wid * 32 * 3840;
#define LQ_X(ST) do { kx = NEXT10(kx); LX_ISSUE(kx, ((ST) & 3) * 16384); } while (0)
#define LQ_W(ST)                                                                                                \
  do {                                                                                                          \
    kw = NEXT10(kw);                                                                                            \
    glds4(vw[0], vw[1], vw[2], vw[3], wb + (size_t)kw * (64 * 3840), lx + WR + ((ST) & 3) * 16384);             \
    glds4(vw[0], vw[1], vw[2], vw[3], wb + (size_t)kw * (64 * 3840) + 16 * 3840, lx + WR + 4096 + ((ST) & 3) * 16384); \
  } while (0)
      int kx = sq0, kw = sq0;
      LQ_X(1); LQ_W(1);
      asm volatile("" : "+v"(vg[0]), "+v"(vg[1]), "+v"(vg[2]), "+v"(vg[3]), "+v"(vg[4]), "+v"(vg[5]));
      WVB(17);
      LQ_X(2); LQ_W(2);
      WVB(16);
      LQ_X(3); LQ_W(3); LQ_X(4);
      WVB(24);
      LQ_W(4); LQ_X(5); LQ_W(5);
#pragma unroll 1
      for (int k = 3; k < 7; ++k) {
        WVB(32);
        LQ_X(k + 3); LQ_W(k + 3);
      }
#undef LQ_X
#undef LQ_W
    }
    const char* wb = (wid ? wu : wg) + fbk * 192;
    const unsigned lw = ldsb + wid * 12288;
#define LG_W(J, WO)                                                                                             \
  do {                                                                                                          \
    glds4(vg[0], vg[1], vg[2], vg[3], wb + (size_t)(J) * (64 * 3072), lw + (WO));                               \
    glds2(vg[4], vg[5], wb + (size_t)(J) * (64 * 3072), lw + 4096 + (WO));                                      \
    glds4(vg[0], vg[1], vg[2], vg[3], wb + (size_t)(J) * (64 * 3072) + 32 * 3072, lw + 6144 + (WO));            \
    glds2(vg[4], vg[5], wb + (size_t)(J) * (64 * 3072) + 32 * 3072, lw + 6144 + 4096 + (WO));                   \
  } while (0)
    int kw = sg0, kx = sg0;
    LG_W(kw, GWS);
    if (has_qkv) { WVB(44); WVB(28); WVB(12); }
    LX_ISSUE(kx, 32768); kx = NEXT10(kx);
    LX_ISSUE(kx, 49152); kx = NEXT10(kx);
    kw = NEXT10(kw); LG_W(kw, GW0 - 24576);
    LX_ISSUE(kx, 0); kx = NEXT10(kx);
    int wo = GW0 - 49152;
#pragma unroll 1
    for (int j = 2; j < 9; ++j) {
      WVB(28);
      kw = NEXT10(kw); LG_W(kw, wo);
      LX_ISSUE(kx, ((j + 3) & 3) * 16384); kx = NEXT10(kx);
      wo = wo == GW0 - 49152 ? GW0 : wo - 24576;
    }
    WVB(28);
    kw = NEXT10(kw); LG_W(kw, GW0);
#undef LG_W
#undef LX_ISSUE
    WVB(20);
    {
      const char* qg = ws + WS_Q + (size_t)head * QHS + (size_t)(qb * 2 + wid) * QGS;
      glds4_sc1(vl, vl, vl, vl, qg, ldsb + SCR_Q + wid * 5120);
      glds1_sc1(vl, qg + 4096, ldsb + SCR_Q + wid * 5120 + 4096);
    }
    AT_ISSUE2(0, AT_A);
    AT_ISSUE2(1, AT_B);
    WVB(22);
  } else if (wid < 4) {
    const int hw = wid - 2, ht = hw * 64 + lane;
    const __amdgpu_buffer_rsrc_t rs = __builtin_amdgcn_make_buffer_rsrc(ws, 0, (int)WS_DBG, 0x00020000);
    __builtin_amdgcn_s_setprio(3);
#define HBAR() do { if (has_qkv) { SBAR(); BAR(); SBAR(); } } while (0)
#define PIN5(V) asm volatile("" : "+v"(V[0]), "+v"(V[1]), "+v"(V[2]), "+v"(V[3]), "+v"(V[4]))
    const bf16x8 ones = {0x3f80, 0x3f80, 0x3f80, 0x3f80, 0x3f80, 0x3f80, 0x3f80, 0x3f80};
    const int r = lane & 15, g = lane >> 4;
    const unsigned t32 = (unsigned)tag;
    char* csd = ws + WS_CS + (size_t)((cb * 16 + sq0) * 128 + hw * 64 + (r >> 2) * 16 + 4 * g + (r & 3)) * 8;
    char* cgd = ws + WS_CG + (size_t)((fbk * 16 + sg0) * 192 + hw * 96 + (r >> 2) * 16 + 4 * g + (r & 3)) * 8;
    unsigned sto = (unsigned)WS_ST + (rb * 128 + ht) * 8;
    unsigned cso = (unsigned)WS_CS + ((has_qkv ? cb : 0) * 16 + hh) * 1024 + hw * 512 + r32 * 16;
    unsigned cgo = (unsigned)WS_CG + fbk * 16 * 1536 + hw * 768 + (lane < 48 ? lane : 0) * 16;
    unsigned kpo = (unsigned)WS_KP + (head * 32 + r32) * 32 + hh * 16;
    const int c0 = (head * 20) >> 7, c1 = (head * 20 + 19) >> 7;
    const char* fkv = ws + WS_FQ + ((5 + (g >> 1) * 5 + ((g & 1) ? c1 : c0)) * 16 + r) * 8;
    const char* fq = ws + WS_FQ + (((lane & 1) ? c1 : c0) * 16 + qb * 2 + hw) * 8;
    lds_ptr wqa[4], wga[2];
#pragma unroll
    for (int nt = 0; nt < 4; ++nt) wqa[nt] = lds + WR + (8 * g + (r >> 2)) * 256 + (((hw * 4 + nt) ^ ((r >> 2) | ((g & 1) << 2))) * 32) + (r & 3) * 8;
#pragma unroll
    for (int i = 0; i < 2; ++i) wga[i] = lds + GWS + hw * 12288 + (8 * g + (r >> 2)) * 192 + ((i ^ (g & 1)) * 32) + (r & 3) * 8;
    asm volatile("" : "+v"(csd), "+v"(cgd), "+v"(sto), "+v"(cso), "+v"(cgo), "+v"(kpo), "+v"(fkv), "+v"(fq), "+v"(wqa[0]), "+v"(wqa[1]), "+v"(wqa[2]), "+v"(wqa[3]),
                 "+v"(wga[0]), "+v"(wga[1]));
    if (has_qkv) {
      SBAR(); WVB(1); SBAR();
      f32x4 cs[4];
#pragma unroll
      for (int nt = 0; nt < 4; ++nt) {
        cs[nt] = MFMA16(cat8(ldstr(wqa[nt]), ldstr(wqa[nt] + 1024)), ones, (f32x4{0.f, 0.f, 0.f, 0.f}));
        cs[nt] = MFMA16(cat8(ldstr(wqa[nt] + 8192), ldstr(wqa[nt] + 9216)), ones, cs[nt]);
      }
      const f32x4 v = (r & 8) ? ((r & 4) ? cs[3] : cs[2]) : ((r & 4) ? cs[1] : cs[0]);
      st_gran(csd, f2u((r & 2) ? ((r & 1) ? v[3] : v[2]) : ((r & 1) ? v[1] : v[0])), tag);
      SBAR(); WLB(); SBAR();
      BAR(); BAR(); BAR(); BAR();
    }
    {
      u32x2 gst = bld8_sc1(rs, sto);
      u32x4 gc[5];
      if (has_qkv) {
#pragma unroll
        for (int i = 0; i < 5; ++i) gc[i] = bld16_sc1(rs, cso + i * 2048);
        BAR(); BAR();
      }
      _Pragma("unroll 1") for (unsigned spins = 0; spins < kSpinCap; ++spins) {
        unsigned bad = gst[1] ^ t32;
        if (has_qkv) {
#pragma unroll
          for (int i = 0; i < 5; ++i) bad |= (gc[i][1] ^ t32) | (gc[i][3] ^ t32);
        }
        if (wall(bad == 0u)) break;
        __builtin_amdgcn_s_sleep(2);
        asm volatile("" ::: "memory");
        gst = bld8_sc1(rs, sto);
        if (has_qkv) {
#pragma unroll
          for (int i = 0; i < 5; ++i) gc[i] = bld16_sc1(rs, cso + i * 2048);
        }
      }
      HBAR();
      *(LAS f32x2*)(lds + SCR_STAT + ht * 8) = f32x2{bflo(gst[0]), 1.0f / bfhi(gst[0])};
      if (has_qkv) {
        float a = 0.f, b = 0.f;
#pragma unroll
        for (int i = 0; i < 5; ++i) { a = adds(a, u2f(gc[i][0])); b = adds(b, u2f(gc[i][2])); }
        a = sumx32(a); b = sumx32(b);
        if (lane < 32) *(LAS f32x2*)(lds + SCR_CS + hw * 256 + lane * 8) = f32x2{a, b};
        WLB();
      }
    }
    BAR();
    {
      f32x4 cs[6];
#pragma unroll
      for (int nt = 0; nt < 6; ++nt) {
        const lds_ptr wa = wga[nt & 1] + (nt >> 1) * 64;
        cs[nt] = MFMA16(cat8(ldstr(wa), ldstr(wa + 768)), ones, (f32x4{0.f, 0.f, 0.f, 0.f}));
        cs[nt] = MFMA16(cat8(ldstr(wa + 6144), ldstr(wa + 6912)), ones, cs[nt]);
      }
      asm volatile("" : "+v"(cs[0]), "+v"(cs[1]), "+v"(cs[2]), "+v"(cs[3]), "+v"(cs[4]), "+v"(cs[5]));
      SBAR(); WLB(); SBAR();
      const f32x4 v = (r & 8) ? ((r & 4) ? cs[3] : cs[2]) : ((r & 4) ? cs[1] : cs[0]), w = (r & 4) ? cs[5] : cs[4];
      st_gran(cgd, f2u((r & 2) ? ((r & 1) ? v[3] : v[2]) : ((r & 1) ? v[1] : v[0])), tag);
      if (r < 8) st_gran(cgd + 512, f2u((r & 2) ? ((r & 1) ? w[3] : w[2]) : ((r & 1) ? w[1] : w[0])), tag);
    }
    BAR();
    if (ht == 0 && has_qkv) set_flag(ws + WS_FQ + (cb * 16 + rb) * 8, tag);
    BAR();
    {
      u32x4 gc[10];
#pragma unroll
      for (int i = 0; i < 10; ++i) gc[i] = bld16_sc1(rs, cgo + i * 1536);
      BAR(); BAR();
      _Pragma("unroll 1") for (unsigned spins = 0; spins < kSpinCap; ++spins) {
        unsigned bad = 0u;
#pragma unroll
        for (int i = 0; i < 10; ++i) bad |= (gc[i][1] ^ t32) | (gc[i][3] ^ t32);
        if (wall(bad == 0u)) break;
        __builtin_amdgcn_s_sleep(2);
        asm volatile("" ::: "memory");
#pragma unroll
        for (int i = 0; i < 10; ++i) gc[i] = bld16_sc1(rs, cgo + i * 1536);
      }
      SBAR(); BAR(); SBAR();
      const u64 f0 = __hip_atomic_load((gu64*)fkv, RLX_AGENT), f1 = __hip_atomic_load((gu64*)fq, RLX_AGENT);
      float a = 0.f, b = 0.f;
#pragma unroll
      for (int i = 0; i < 10; ++i) { a = adds(a, u2f(gc[i][0])); b = adds(b, u2f(gc[i][2])); }
      if (lane < 48) *(LAS f32x2*)(lds + SCR_CG + hw * 384 + lane * 8) = f32x2{a, b};
      SBAR(); BAR(); SBAR();
      if (!wall(f0 == tag && f1 == tag)) wait_tag(fkv, fq, tag);
      __builtin_amdgcn_fence(__ATOMIC_ACQUIRE, "wavefront");
    }
    {
      const u32x4 kp = bld16_sc1(rs, kpo);
      BAR();
      const float ks = hh ? u2f(kp[0]) : adds(adds(u2f(kp[0]), u2f(kp[1])), adds(u2f(kp[2]), u2f(kp[3])));
      const float kn2 = maxx16(max16(sumx32(ks)));
      if (ht == 0) *(LAS float*)(lds + SCR_KN) = kn2;
      WLB();
    }
  } else {
    const int cw = wid - 4;
    __builtin_amdgcn_s_setprio(2);
    Pend pd = {};
    pd.base = ws;
    if (has_qkv) {
      if (cb < 10) qkv_compute<true>(lds, cw, lane, rb, cb, ws, pd TPASS);
      else qkv_compute<false>(lds, cw, lane, rb, cb, ws, pd TPASS);
    }
    gu_compute(lds, cw, lane, rb, fbk, ws, pd, has_qkv TPASS);
    __builtin_amdgcn_s_setprio(0);
  }
  {
    const lds_ptr qp = lds + SCR_Q + ((wid >> 1) & 1) * 5120 + (wid & 1) * 512 + r32 * 16;
    const u32x4 l0 = __builtin_bit_cast(u32x4, lds128(qp + hh * 2048)), l1 = __builtin_bit_cast(u32x4, lds128(qp + hh * 2048 + 1024));
    const u32x4 qbb = __builtin_bit_cast(u32x4, lds128(qp + 4096));
#pragma unroll
    for (int a = 0; a < 2; ++a) {
      qa[a] = __builtin_bit_cast(bf16x8, u32x4{l0[2 * a], l0[2 * a + 1], l1[2 * a], l1[2 * a + 1]});
#pragma unroll
      for (int j = 0; j < 2; ++j) { u32x2 u = {hh == j ? qbb[2 * a] : 0u, hh == j ? qbb[2 * a + 1] : 0u}; qv[a][j] = __builtin_bit_cast(s16x4, u); }
    }
    LAS f32x2* mp = (LAS f32x2*)(lds + SCR_M + (wid & 3) * 256 + r32 * 8);
    if (wid < 4) {
      const float kn2 = *(const LAS float*)(lds + SCR_KN);
#pragma unroll
      for (int a = 0; a < 2; ++a) {
        const unsigned w[5] = {hh ? qbb[2 * a + 1] : qbb[2 * a], l0[2 * a], l0[2 * a + 1], l1[2 * a], l1[2 * a + 1]};
        float ss = 0.f;
#pragma unroll
        for (int e = 0; e < 5; ++e) { ss = fmas(bflo(w[e]), bflo(w[e]), ss); ss = fmas(bfhi(w[e]), bfhi(w[e]), ss); }
        mrow[a] = __builtin_amdgcn_sqrtf(fmas(sumx32(ss), kn2 * 1.04f, 0.f));
      }
      *mp = f32x2{mrow[0], mrow[1]};
      WVB(0);
      __builtin_amdgcn_s_setprio(0);
    } else {
      const f32x2 m = *mp;
      mrow[0] = m[0]; mrow[1] = m[1];
    }
  }

  const int kh = wid >> 2;
#define D_ISSUE(ST, XP, XS)                                                                                    \
  do {                                                                                                         \
    const unsigned d_ = rfl(dl0 + ((ST) & 3) * D_STG);                                                         \
    glds4_sc1(vl, vl + (XS) - 1024, vl + 2 * ((XS) - 1024), vl + 3 * ((XS) - 1024), XP, d_);                   \
    glds4_sc1(vl, vl, vl, vl, wpb + (size_t)(ST) * 20480, d_ + 4096);                                          \
    glds1_sc1(vl, wpb + (size_t)(ST) * 20480 + 4096, d_ + 8192);                                               \
  } while (0)
#define D_ISSUE_H(ST) D_ISSUE(ST, hb + (ST) * 4096, 192 * 256)
#define FRESH()                                                                                                \
  int lane_ = lane, bid_ = bid, wq = wid & 3; char* wz = ws;                                                   \
  asm volatile("" : "+v"(lane_));                                                                              \
  asm volatile("" : "+s"(bid_), "+s"(wz), "+s"(wq));                                                           \
  const int xc = bid_ & 7, ix = bid_ >> 3, mb = (xc >> 1) * 8 + (ix & 7), nb = (xc & 1) * 4 + (ix >> 3);       \
  const unsigned vl = lane_ * 16, dl0 = ldsb + wq * 9216;                                                      \
  const char* wpb = wz + WS_WP + (size_t)(nb * 68 + wq) * 5120;                                                \
  const char* hb = wz + WS_H + (size_t)mb * 4 * 192 * 256 + wq * 1024;                                         \
  const char* kvg = wz + WS_KV + (size_t)(xc * 4 + (ix >> 3)) * KVHS;                                          \
  (void)vl; (void)dl0; (void)wpb; (void)hb; (void)kvg
  f32x4 o01[2][2], o2; float lpart;
  {
    const int dsel = r32 <= 20 ? r32 : (r32 < 28 ? 20 : 4);
    const int koff = kh * (int)KVGS + hh * 2048 + r32 * 16, kboff = kh * (int)KVGS + 4096 + lane * 16, voff = kh * (int)KVGS + 5120 + hh * 336 + dsel * 16;
    for (int pass = 0;; ++pass) {
      f32x16 negm[2], sc[2], oacc[2];
#pragma unroll
      for (int a = 0; a < 2; ++a)
#pragma unroll
        for (int e = 0; e < 16; ++e) { negm[a][e] = -mrow[a]; oacc[a][e] = 0.f; }
      asm volatile("" : "+v"(negm[0]), "+v"(negm[1]));
      u32x4 pw[2][2] = {};
      bf16x8 ka, kb, vf[2] = {};
      int sl_cur = AT_A, sl_next = AT_B;
      unsigned hook = wid < 4 ? 0xffu : 0xcau;
      u64 tbl = (u64)(AT_CH >> 8) | (u64)(2 * AT_CH >> 8) << 10 | (u64)(3 * AT_CH >> 8) << 20 | (u64)(AT_A >> 8) << 30 | (u64)(AT_6 >> 8) << 40 | (u64)(AT_7 >> 8) << 50;
      asm volatile("" : "+s"(hook), "+s"(tbl));
#define LDKA(P, t) cat8(lds64((P) + ((t) >> 1) * 512 + ((t) & 1) * 8), lds64((P) + 1024 + ((t) >> 1) * 512 + ((t) & 1) * 8))
      ka = LDKA(lds + koff + AT_A, 0);
      kb = lds128(lds + kboff + AT_A);
#define KBH(t) (((t) & 1) ? __builtin_shufflevector(kb, kb, 4, 5, 6, 7) : __builtin_shufflevector(kb, kb, 0, 1, 2, 3))
      sc[0] = MFMA32(ka, qa[0], negm[0]);
      sc[0] = MFMA32K8(KBH(0), qv[0][0], sc[0]);
#define EX4(X, i) do { X[i] = __builtin_amdgcn_exp2f(X[i]); X[i + 1] = __builtin_amdgcn_exp2f(X[i + 1]); X[i + 2] = __builtin_amdgcn_exp2f(X[i + 2]); X[i + 3] = __builtin_amdgcn_exp2f(X[i + 3]); } while (0)
#define PK2(P, j, X, i) do { P[j] = cvtpk(X[i], X[i + 1]); P[j + 1] = cvtpk(X[i + 2], X[i + 3]); } while (0)
#define PFR(a, s) __builtin_bit_cast(bf16x8, pw[a][s])
#define UNIT(A, B, TB, LDK, LDV)                                                             \
  do {                                                                                       \
    sc[B] = MFMA32(ka, qa[B], negm[B]); SBAR();                                              \
    EX4(sc[A], 0); PIN(sc[A]); SBAR();                                                       \
    sc[B] = MFMA32K8(KBH(TB), qv[B][(TB) >> 1], sc[B]); SBAR();                              \
    LDK; SBAR();                                                                             \
    EX4(sc[A], 4); PK2(pw[A][0], 0, sc[A], 0); PIN(sc[A]); PIN(pw[A][0]); SBAR();            \
    oacc[B] = MFMA32(vf[0], PFR(B, 0), oacc[B]); SBAR();                                     \
    EX4(sc[A], 8); PK2(pw[A][0], 2, sc[A], 4); PIN(sc[A]); PIN(pw[A][0]); SBAR();            \
    oacc[B] = MFMA32(vf[1], PFR(B, 1), oacc[B]); SBAR();                                     \
    LDV; SBAR();                                                                             \
    EX4(sc[A], 12); PK2(pw[A][1], 0, sc[A], 8); PK2(pw[A][1], 2, sc[A], 12); PIN(pw[A][1]); SBAR(); \
  } while (0)
#define LDV_(t) do { vf[0] = lds128(vp + (t) * 1344); vf[1] = lds128(vp + (t) * 1344 + 672); } while (0)
      for (int c = 0; c < 8; ++c) {
        lds_ptr kp_ = lds + koff + sl_cur, vp = lds + voff + sl_cur, kn = lds + koff + sl_next, kbn = lds + kboff + sl_next;
        UNIT(0, 1, 0, ka = LDKA(kp_, 1), LDV_(0));
        UNIT(1, 0, 1, (void)0, (void)0);
        UNIT(0, 1, 1, ka = LDKA(kp_, 2), LDV_(1));
        UNIT(1, 0, 2, (void)0, (void)0);
        UNIT(0, 1, 2, ka = LDKA(kp_, 3), LDV_(2));
        UNIT(1, 0, 3, (void)0, (void)0);
        if (__builtin_expect((hook >> c) & 1, 0)) {
         if (c == 7) WLB();
         else if ((0x4a >> c) & 1) WVB(0);
         if (wid < 4) {
          FRESH();
          if (c == 0) {
            AT_ISSUE(2, AT_CH); AT_ISSUE(3, 2 * AT_CH);
        if (pass == 0 && bid_ < 136) {
          const int ks = bid_ >> 1;
          unsigned v[5];
#pragma unroll
          for (int j = 0; j < 5; ++j) {
            const int u = j * 64 + lane_, pr = u / 10, pos = u - pr * 10, k = pr ^ (((pr >> 3) & 1) << 2);
            const int row = ks < 48 ? k : ks < 64 ? (k >> 4) * 20 + (k & 15) : (k >> 2) * 20 + 16 + (k & 3);
            v[j] = row * 1280 + pos * 16 - 1024 * (j & 3);
          }
          const char* src = ks < 48 ? wd + (size_t)ks * 32 * 1280 : ks < 64 ? wo + (size_t)(ks - 48) * 40 * 1280 : wo + (size_t)(ks - 64) * 160 * 1280;
          glds5(v, rflp(src + ((bid_ & 1) * 4 + wq) * 160), rfl(ldsb + AT_RP + wq * 5120));
        }
          } else if (c == 2) {
            AT_ISSUE(6, AT_6);
          } else if (c == 7) {
            D_ISSUE_H(2);
          } else if (c == 1) {
            AT_ISSUE(4, 3 * AT_CH); AT_ISSUE(5, AT_A);
            if (pass == 0) {
              if (wq == 0 && lane_ == 0) {
                const int t_ = (xc & 1) * 30 + ix;
                set_flag(wz + WS_FH + (((xc >> 1) * 4 + (t_ & 3)) * 16 + (xc & 1) * 8 + (ix >> 2)) * 8, tag);
              }
              if (bid_ < 136) {
                u32x4 t[5];
#pragma unroll
                for (int j = 0; j < 5; ++j) {
                  const int r = lane_ & 15, g = lane_ >> 4;
                  const lds_ptr w0 = lds + AT_RP + wq * 5120 + (8 * g + ((r >> 2) ^ (4 * (g & 1)))) * 160 + (r & 3) * 8 + j * 32;
                  const lds_ptr w1 = lds + AT_RP + wq * 5120 + (8 * g + ((4 + (r >> 2)) ^ (4 * (g & 1)))) * 160 + (r & 3) * 8 + j * 32;
                  t[j] = __builtin_bit_cast(u32x4, cat8(ldstr(w0), ldstr(w1)));
                }
#pragma unroll
                for (int j = 0; j < 5; ++j) st16_sc1(wz + WS_WP + (size_t)(((bid_ & 1) * 4 + wq) * 68 + (bid_ >> 1)) * 5120, vl + j * 1024, t[j]);
              }
            }
          } else if (c == 3) {
            AT_ISSUE(7, AT_7);
            if (pass == 0 && bid_ < 136 && wq == 0 && lane_ == 0) set_flag(wz + WS_FW + ((bid_ & 1) * 128 + (bid_ >> 1)) * 8, tag);
          } else if (c == 4) {
            glds1_sc1((unsigned)WS_FH + (xc >> 1) * 512 + (lane_ & 31) * 16, wz, rfl(ldsb + AT_FL + 1024 + wq * 2048));
            glds1_sc1((unsigned)WS_FW + (xc & 1) * 1024 + (lane_ < 34 ? lane_ : 0) * 16, wz, rfl(ldsb + AT_FL + 2048 + wq * 2048));
            if (bid_ == 255 && wq == 0) { glds1_sc1(vl, wz + WS_FH, rfl(ldsb + AT_FL + 10240)); glds1_sc1(vl, wz + WS_FH + 1024, rfl(ldsb + AT_FL + 11264)); }
          } else if (c == 5) {
            asm volatile("s_waitcnt vmcnt(0)" ::: "memory");
            const u32x4 f0 = __builtin_bit_cast(u32x4, lds128(lds + AT_FL + 1024 + wq * 2048 + vl));
            const u32x4 f1 = __builtin_bit_cast(u32x4, lds128(lds + AT_FL + 2048 + wq * 2048 + vl));
            const unsigned tl = (unsigned)tag, th = (unsigned)(tag >> 32);
            if (!wall(f0[0] == tl && f0[1] == th && f0[2] == tl && f0[3] == th && f1[0] == tl && f1[1] == th && f1[2] == tl && f1[3] == th)) {
              wait_tag(wz + WS_FH + ((xc >> 1) * 64 + lane_) * 8, wz + WS_FW + ((xc & 1) * 128 + lane_) * 8, tag);
              wait_tag(wz + WS_FW + ((xc & 1) * 128 + 64 + (lane_ & 3)) * 8, tag);
            }
            if (bid_ == 255 && wq == 0) {
              const u32x4 a = __builtin_bit_cast(u32x4, lds128(lds + AT_FL + 10240 + vl)), b = __builtin_bit_cast(u32x4, lds128(lds + AT_FL + 11264 + vl));
              if (!wall(a[0] == tl && a[1] == th && a[2] == tl && a[3] == th && b[0] == tl && b[1] == th && b[2] == tl && b[3] == th)) {
#pragma unroll 1
                for (int i = 0; i < 4; ++i) wait_tag(wz + WS_FH + (i * 64 + lane_) * 8, tag);
              }
              if (lane_ == 0) set_flag(wz + WS_EP, tag);
            }
            const unsigned lt = (wq * 64 + lane_) * 128;
            const char* hp = wz + WS_H + (size_t)((xc >> 1) * 32 + ix) * 49152;
            const char* wp = wz + WS_WP + (size_t)(xc & 1) * 4 * 68 * 5120 + (size_t)ix * 43520;
            touch_sc1(lt, hp, rfl(ldsb + AT_FL)); touch_sc1(lt, wp, rfl(ldsb + AT_FL));
            if (wq < 2) { touch_sc1(lt, hp + 32768, rfl(ldsb + AT_FL)); touch_sc1(lt < 43520 - 32768 ? lt : 0u, wp + 32768, rfl(ldsb + AT_FL)); }
          } else {
            D_ISSUE_H(0); D_ISSUE_H(1);
          }
         }
        }
        SBAR();
        UNIT(0, 1, 3, do { ka = LDKA(kn, 0); kb = lds128(kbn); } while (0), LDV_(3));
        UNIT(1, 0, 0, (void)0, (void)0);
        sl_cur = sl_next;
        sl_next = ((unsigned)tbl & 1023u) << 8;
        tbl >>= 10;
      }
      oacc[1] = MFMA32(vf[0], PFR(1, 0), oacc[1]);
      oacc[1] = MFMA32(vf[1], PFR(1, 1), oacc[1]);
#pragma unroll
      for (int e = 0; e < 4; ++e) {
        auto sw = __builtin_amdgcn_permlane32_swap(f2u(oacc[0][8 + e]), f2u(oacc[1][8 + e]), false, false);
        o2[e] = u2f(sw[0]);
        if (e == 0) lpart = u2f(sw[1]);
      }
#pragma unroll
      for (int a = 0; a < 2; ++a)
#pragma unroll
        for (int i = 0; i < 2; ++i) o01[a][i] = f32x4{oacc[a][4 * i], oacc[a][4 * i + 1], oacc[a][4 * i + 2], oacc[a][4 * i + 3]};
      if (kh) {
        const lds_ptr ex = lds + (wid == 7 ? 142080 : 147712 + (wid - 4) * 5376);
        *(LAS f32x4*)(ex + vl) = o01[0][0]; *(LAS f32x4*)(ex + 1024 + vl) = o01[0][1];
        *(LAS f32x4*)(ex + 2048 + vl) = o01[1][0]; *(LAS f32x4*)(ex + 3072 + vl) = o01[1][1];
        *(LAS f32x4*)(ex + 4096 + vl) = o2; *(LAS float*)(ex + 5120 + lane * 4) = lpart;
      }
      const bool anybad = wany(!(lpart >= 0x1p-40f && lpart <= 0x1p40f));
      if (lane == 0) *(LAS unsigned*)(lds + AT_BAD + wid * 4) = (pass == 0 && anybad) ? 1u : 0u;
      WVB(9);
      const u32x4 fa = *(const LAS u32x4*)(lds + AT_BAD), fb = *(const LAS u32x4*)(lds + AT_BAD + 16);
      if (!rfl(fa[0] | fa[1] | fa[2] | fa[3] | fb[0] | fb[1] | fb[2] | fb[3])) break;
      FRESH();
      WVB(0);
      if (wid < 4) { AT_ISSUE(0, AT_A); AT_ISSUE(1, AT_B); }
      float mx[2] = {-INFINITY, -INFINITY};
      int lane = threadIdx.x & 63;
      PIN(lane);
      const int r32 = lane & 31, hh = lane >> 5;
      for (int gi = 0; gi < 16; ++gi) {
        const char* gp = kvg + (size_t)gi * KVGS;
        kb = __builtin_bit_cast(bf16x8, ld16_sc1_wait(gp + 4096 + lane * 16));
#pragma unroll
        for (int tp = 0; tp < 2; ++tp) {
          const u32x4 k0 = ld16_sc1_wait(gp + hh * 2048 + r32 * 16 + tp * 512), k1 = ld16_sc1_wait(gp + hh * 2048 + r32 * 16 + 1024 + tp * 512);
#pragma unroll
          for (int u = 0; u < 2; ++u) {
            ka = __builtin_bit_cast(bf16x8, u32x4{k0[2 * u], k0[2 * u + 1], k1[2 * u], k1[2 * u + 1]});
#pragma unroll
            for (int a = 0; a < 2; ++a) {
              f32x16 s = MFMA32(ka, qa[a], f32x16{});
              s = MFMA32K8(KBH(u), qv[a][tp], s);
#pragma unroll
              for (int e = 0; e < 16; ++e) mx[a] = fmaxf(mx[a], s[e]);
            }
          }
        }
      }
#pragma unroll
      for (int a = 0; a < 2; ++a) mrow[a] = maxx32(mx[a]);
      WVB(0);
    }
  }
#undef AT_ISSUE
#undef UNIT

  {
  FRESH();
  if (wid < 4) {
    __builtin_amdgcn_s_setprio(3);
    {
      const lds_ptr ex = lds + (wq == 3 ? 142080 : 147712 + wq * 5376);
      f32x4 p[5];
#pragma unroll
      for (int i = 0; i < 5; ++i) p[i] = *(const LAS f32x4*)(ex + i * 1024 + vl);
      float lp = *(const LAS float*)(ex + 5120 + lane_ * 4);
      asm volatile("s_waitcnt lgkmcnt(0)" : "+v"(p[0]), "+v"(p[1]), "+v"(p[2]), "+v"(p[3]), "+v"(p[4]), "+v"(lp)::"memory");
      D_ISSUE_H(3);
      const int head = xc * 4 + (ix >> 3), qb = ix & 7, r32 = lane_ & 31, hh = lane_ >> 5;
      const float rl = __builtin_amdgcn_rcpf(adds(lpart, lp));
      auto sr = __builtin_amdgcn_permlane32_swap(f2u(rl), f2u(rl), false, false);
#pragma unroll
      for (int a = 0; a < 2; ++a) {
        const float ra = u2f(sr[a]);
        unsigned w[4];
#pragma unroll
        for (int i = 0; i < 2; ++i)
#pragma unroll
          for (int e = 0; e < 2; ++e)
            w[2 * i + e] = cvtpk(muls(adds(o01[a][i][2 * e], p[2 * a + i][2 * e]), ra), muls(adds(o01[a][i][2 * e + 1], p[2 * a + i][2 * e + 1]), ra));
        auto s0 = __builtin_amdgcn_permlane32_swap(w[0], w[2], false, false);
        auto s1 = __builtin_amdgcn_permlane32_swap(w[1], w[3], false, false);
        const int tok = qb * 256 + wq * 64 + a * 32 + r32;
        st16_sc1(wz + WS_O, (((tok >> 4) * 80 + head * 2 + hh) * 16 + (tok & 15)) * 16, u32x4{s0[0], s1[0], s0[1], s1[1]});
      }
      const int tok = qb * 256 + wq * 64 + lane_;
      st8_sc1(wz + WS_O + ((size_t)((tok >> 4) * 80 + 64 + (head >> 1)) * 16 + (tok & 15)) * 16 + (head & 1) * 8,
              u32x2{cvtpk(muls(adds(o2[0], p[4][0]), rl), muls(adds(o2[1], p[4][1]), rl)), cvtpk(muls(adds(o2[2], p[4][2]), rl), muls(adds(o2[3], p[4][3]), rl))});
    }
    const char* ob = wz + WS_O + (size_t)mb * 4 * 80 * 256 + wq * 1024;
    WVB(12);
    D_ISSUE_H(4);
    WVB(12);
    D_ISSUE_H(5);
    asm volatile("s_waitcnt vmcnt(18)" ::: "memory");
    if (lane_ == 0) set_flag(wz + WS_FO4 + (((ix & 7) * 4 + wq) * 32 + xc * 4 + (ix >> 3)) * 8, tag);
    D_ISSUE_H(6);
    const char* fop = wz + WS_FO4 + mb * 256;
#pragma unroll 1
    for (int k = 4; k <= 8; ++k) {
      WVB(18);
      if (k == 4)
    {
      unsigned v[3];
#pragma unroll
      for (int j = 0; j < 3; ++j) { const int u = min(j * 64 + lane_, 159), row = u / 10; v[j] = row * 1280 + (u - row * 10) * 16; }
      const char* xp = x + (size_t)(mb * 64 + wq * 16) * 1280 + nb * 160;
      glds2(v[0], v[1] - 1024, xp, rfl(ldsb + SCR + 4096 + wq * 3072));
      glds1_sc1(v[2], xp, rfl(ldsb + SCR + 4096 + wq * 3072 + 2048));
    }
      if (k == 6) glds1_sc1((lane_ & 15) * 16, fop, rfl(ldsb + SCR + wq * 1024));
      D_ISSUE_H(k + 3);
    }
    {
      const u32x4 f = __builtin_bit_cast(u32x4, lds128(lds + SCR + wq * 1024 + vl));
      const unsigned tl = (unsigned)tag, th = (unsigned)(tag >> 32);
      if (!wall(f[0] == tl && f[1] == th && f[2] == tl && f[3] == th)) wait_tag(fop + (lane_ & 31) * 8, tag);
    }
#pragma unroll 1
    for (int j = 0; j < 5; ++j) { WVB(18); D_ISSUE(12 + j, ob + j * 4096, 80 * 256); }
    WVB(18);
    WVB(9);
    WVB(0);
    BAR();
  } else {
    const int r = lane_ & 15, g = lane_ >> 4;
    const lds_ptr xa0 = lds + (wq & 1) * 9216 + wq * 1024 + vl, xa1 = lds + (wq & 1) * 9216 + (wq ^ 1) * 1024 + vl, wa = lds + (wq & 1) * 9216 + 4096 + vl;
    f32x4 acc[5], acs[5];
#pragma unroll
    for (int i = 0; i < 5; ++i) { acc[i] = f32x4{0.f, 0.f, 0.f, 0.f}; acs[i] = f32x4{0.f, 0.f, 0.f, 0.f}; }
    bf16x8 xf[2][2][2], wf[2][2][5];
#define D_READ(B, SO)                                                                                  \
  do {                                                                                                 \
    _Pragma("unroll") for (int j = 0; j < 2; ++j) {                                                    \
      xf[B][j][0] = lds128(xa0 + (SO) + j * 18432); xf[B][j][1] = lds128(xa1 + (SO) + j * 18432);      \
      _Pragma("unroll") for (int i = 0; i < 5; ++i) wf[B][j][i] = lds128(wa + (SO) + j * 18432 + i * 1024); \
    }                                                                                                  \
  } while (0)
#define D_MMA(B)                                                                                       \
  do {                                                                                                 \
    _Pragma("unroll") for (int j = 0; j < 2; ++j)                                                      \
      _Pragma("unroll") for (int nt = 0; nt < 5; ++nt) {                                               \
        acs[nt] = MFMA16(wf[B][j][nt], xf[B][j][1], acs[nt]);                                          \
        acc[nt] = MFMA16(wf[B][j][nt], xf[B][j][0], acc[nt]);                                          \
      }                                                                                                \
  } while (0)
#define D_MIX() do { _Pragma("unroll") for (int i = 0; i < 14; ++i) { SGB(0x8, 1); SGB(0x100, 1); } SGB(0x8, 6); } while (0)
    D_READ(0, 0);
    int so = D_STG;
#pragma unroll 1
    for (int st = 0; st < 16; st += 2) {
      if (st) WLB();
      SBAR();
      D_READ(1, so); D_MMA(0); D_MIX();
      so = so == 3 * D_STG ? 0 : so + D_STG;
      SBAR();
      WLB();
      SBAR();
      D_READ(0, so); D_MMA(1); D_MIX();
      so = so == 3 * D_STG ? 0 : so + D_STG;
      SBAR();
    }
    u32x2 xres[5];
#pragma unroll
    for (int nt = 0; nt < 5; ++nt) xres[nt] = *(const LAS u32x2*)(lds + SCR + 4096 + wq * 3072 + r * 160 + g * 8 + nt * 32);
    D_MMA(0);
#pragma unroll
    for (int nt = 0; nt < 5; ++nt) *(LAS f32x4*)(lds + 40960 + (wq ^ 1) * 5120 + nt * 1024 + vl) = acs[nt];
    WLB();
#pragma unroll
    for (int nt = 0; nt < 5; ++nt) acc[nt] += *(const LAS f32x4*)(lds + 40960 + wq * 5120 + nt * 1024 + vl);
    unsigned w[5][2];
#pragma unroll
    for (int nt = 0; nt < 5; ++nt) {
      w[nt][0] = cvtpk(bflo(xres[nt][0]) + acc[nt][0], bfhi(xres[nt][0]) + acc[nt][1]);
      w[nt][1] = cvtpk(bflo(xres[nt][1]) + acc[nt][2], bfhi(xres[nt][1]) + acc[nt][3]);
    }
    char* op = out + ((size_t)((mb * 64 + wq * 16 + r) * 640 + nb * 80)) * 2;
#pragma unroll
    for (int j = 0; j < 2; ++j) {
      auto s0 = __builtin_amdgcn_permlane16_swap(w[2 * j][0], w[2 * j + 1][0], false, false);
      auto s1 = __builtin_amdgcn_permlane16_swap(w[2 * j][1], w[2 * j + 1][1], false, false);
      *(u32x4*)(op + j * 64 + (g & 1) * 32 + (g >> 1) * 16) = u32x4{s0[0], s1[0], s0[1], s1[1]};
    }
    *(u32x2*)(op + 128 + g * 8) = u32x2{w[4][0], w[4][1]};
  }
  }
}

extern "C" void kernel_launch(void* const* d_in, const int* in_sizes, int n_in, void* d_out, int out_size, void* d_ws, size_t ws_size,
                              hipStream_t stream) {
  fused_block<<<256, 512, 0, stream>>>((const char*)d_in[0], (const char*)d_in[1], (const char*)d_in[2], (const char*)d_in[4],
                                       (const char*)d_in[5], (const char*)d_in[6], (char*)d_ws, (char*)d_out);
}
```

```cpp
#include <hip/hip_runtime.h>
#include <hip/hip_bf16.h>
#include <stdint.h>

#define DI __device__ __forceinline__
#define LAS __attribute__((address_space(3)))
#define GAS __attribute__((address_space(1)))
typedef short bf16x8 __attribute__((ext_vector_type(8)));
typedef short s16x4 __attribute__((ext_vector_type(4)));
typedef float f32x4 __attribute__((ext_vector_type(4)));
typedef float f32x2 __attribute__((ext_vector_type(2)));
typedef float f32x16 __attribute__((ext_vector_type(16)));
typedef unsigned u32x4 __attribute__((ext_vector_type(4)));
typedef unsigned u32x2 __attribute__((ext_vector_type(2)));
typedef __bf16 bf16x2_t __attribute__((ext_vector_type(2)));
typedef LAS char* lds_ptr;
typedef unsigned long long u64;
typedef GAS u64 gu64;

constexpr int S = 2048, D = 640, F = 1536;
constexpr size_t QGS = 5120, QHS = 16 * QGS;
constexpr size_t KVGS = 10752, KVHS = 16 * KVGS;
constexpr size_t WS_EP = 0;
constexpr size_t WS_FQ = 4096;
constexpr size_t WS_FH = WS_FQ + 2048;
constexpr size_t WS_FO = WS_FH + 2048;
constexpr size_t WS_FW = WS_FO + 2048;
constexpr size_t WS_FO4 = 1032192;
constexpr size_t WS_ST = 16384;
constexpr size_t WS_CG = WS_ST + 2048 * 8;
constexpr size_t WS_KP = WS_CG + 16 * 3072 * 8;
constexpr size_t WS_CS = WS_KP + 32 * 32 * 8 * 4;
constexpr size_t WS_Q = 1048576;
constexpr size_t WS_KV = WS_Q + 32 * QHS;
constexpr size_t WS_H = WS_KV + 32 * KVHS + 4096;
constexpr size_t WS_O = WS_H + (size_t)S * F * 2;
constexpr size_t WS_WP = WS_O + (size_t)S * D * 2;
static_assert(WS_CS + 15 * 16 * 128 * 8 <= WS_FO4 && WS_FO4 + 8192 <= WS_Q, "ws map");

#define SBAR() __builtin_amdgcn_sched_barrier(0)
#define PIN(x) asm volatile("" : "+v"(x))
constexpr size_t WS_DBG = (size_t)128 << 20;
#define TS() do {} while (0)
#define TARGS
#define TPASS
#define WVB(N) wait_vm_bar_<N>()
#define WLB() wait_lgkm_bar_()
#define BAR() bar_()
#define MFMA16(a, b, c) __builtin_amdgcn_mfma_f32_16x16x32_bf16(a, b, c, 0, 0, 0)
#define MFMA32(a, b, c) __builtin_amdgcn_mfma_f32_32x32x16_bf16(a, b, c, 0, 0, 0)
#define MFMA32K8(a, b, c) __builtin_amdgcn_mfma_f32_32x32x8bf16_1k(a, b, c, 0, 0, 0)
#define RLX_AGENT __ATOMIC_RELAXED, __HIP_MEMORY_SCOPE_AGENT
#define NOPK 0
#define CODE_LINES 284
#define DPRIO 1
#define DOSE 0
constexpr unsigned kSpinCap = 1u << 17;
constexpr int LDS_BYTES = 163840;

DI unsigned f2u(float x) { return __builtin_bit_cast(unsigned, x); }
DI float u2f(unsigned x) { return __builtin_bit_cast(float, x); }
DI bool wall(bool p) { return __builtin_amdgcn_ballot_w64(!p) == 0; }
DI bool wany(bool p) { return __builtin_amdgcn_ballot_w64(p) != 0; }
DI float adds(float a, float b) { float r; asm("v_add_f32 %0, %1, %2" : "=v"(r) : "v"(a), "v"(b)); return r; }
DI float muls(float a, float b) { float r; asm("v_mul_f32 %0, %1, %2" : "=v"(r) : "v"(a), "v"(b)); return r; }
DI float fmas(float a, float b, float c) { float r; asm("v_fma_f32 %0, %1, %2, %3" : "=v"(r) : "v"(a), "v"(b), "v"(c)); return r; }
DI unsigned rfl(unsigned x) { return __builtin_amdgcn_readfirstlane(x); }
DI const char* rflp(const char* p) { const u64 u = (u64)p; return (const char*)(((u64)rfl((unsigned)(u >> 32)) << 32) | rfl((unsigned)u)); }
DI unsigned cvtpk(float lo, float hi) { f32x2 v = {lo, hi}; bf16x2_t b = __builtin_convertvector(v, bf16x2_t); return __builtin_bit_cast(unsigned, b); }
DI float bfr(float x) { return u2f(cvtpk(x, 0.f) << 16); }
DI float bflo(unsigned u) { return u2f(u << 16); }
DI float bfhi(unsigned u) { return u2f(u & 0xffff0000u); }
DI bf16x8 lds128(lds_ptr p) { return *(const LAS bf16x8*)p; }
DI s16x4 lds64(lds_ptr p) { return *(const LAS s16x4*)p; }
DI s16x4 ldstr(lds_ptr p) { return __builtin_amdgcn_ds_read_tr16_b64_v4i16((LAS s16x4*)p); }
DI bf16x8 cat8(s16x4 a, s16x4 b) { return __builtin_shufflevector(a, b, 0, 1, 2, 3, 4, 5, 6, 7); }
template <int N> DI void wait_vm_bar_() { asm volatile("s_waitcnt vmcnt(%0) lgkmcnt(0)\n\ts_barrier" ::"i"(N) : "memory"); }
DI void wait_lgkm_bar_() { asm volatile("s_waitcnt lgkmcnt(0)\n\ts_barrier" ::: "memory"); }
DI void st16_sc1(char* p, u32x4 v) { asm volatile("global_store_dwordx4 %0, %1, off sc1\n\ts_nop 1" ::"v"(p), "v"(v) : "memory"); }
DI void st16_sc1(const char* sb, unsigned off, u32x4 v) { asm volatile("global_store_dwordx4 %0, %1, %2 sc1\n\ts_nop 1" ::"v"(off), "v"(v), "s"(sb) : "memory"); }
DI void st8_sc1(char* p, u32x2 v) { asm volatile("global_store_dwordx2 %0, %1, off sc1\n\ts_nop 1" ::"v"(p), "v"(v) : "memory"); }
DI u32x4 ld16_sc1_wait(const char* p) { u32x4 v; asm volatile("global_load_dwordx4 %0, %1, off sc1\n\ts_waitcnt vmcnt(0)" : "=&v"(v) : "v"(p) : "memory"); return v; }

DI void glds2(unsigned v0, unsigned v1, const void* sbase, unsigned m0v) {
  unsigned sv;
  asm volatile("s_mov_b32 %0, m0\n\ts_mov_b32 m0, %4\n\ts_nop 0\n\t"
               "global_load_lds_dwordx4 %1, %3\n\t"
               "global_load_lds_dwordx4 %2, %3 offset:1024\n\t"
               "s_mov_b32 m0, %0"
               : "=&s"(sv) : "v"(v0), "v"(v1), "s"(sbase), "s"(m0v) : "memory");
}
DI void glds5(const unsigned (&v)[5], const void* sbase, unsigned m0v) {
  unsigned sv;
  asm volatile("s_nop 4\n\ts_mov_b32 %0, m0\n\ts_mov_b32 m0, %7\n\ts_nop 0\n\t"
               "global_load_lds_dwordx4 %1, %6\n\t"
               "global_load_lds_dwordx4 %2, %6 offset:1024\n\t"
               "global_load_lds_dwordx4 %3, %6 offset:2048\n\t"
               "global_load_lds_dwordx4 %4, %6 offset:3072\n\t"
               "s_add_u32 m0, m0, 4096\n\ts_nop 0\n\t"
               "global_load_lds_dwordx4 %5, %6\n\t"
               "s_mov_b32 m0, %0"
               : "=&s"(sv) : "v"(v[0]), "v"(v[1]), "v"(v[2]), "v"(v[3]), "v"(v[4]), "s"(sbase), "s"(m0v) : "memory", "scc");
}
DI void wait_tag(const char* p0, const char* p1, u64 tag) {
  _Pragma("unroll 1") for (unsigned spins = 0; spins < kSpinCap; ++spins) {
    const u64 a = __hip_atomic_load((gu64*)p0, RLX_AGENT), b = __hip_atomic_load((gu64*)p1, RLX_AGENT);
    if (wall(a == tag && b == tag)) break;
    __builtin_amdgcn_s_sleep(2);
  }
  __builtin_amdgcn_fence(__ATOMIC_ACQUIRE, "wavefront");
}
DI void wait_tag(const char* p0, u64 tag) {
  _Pragma("unroll 1") for (unsigned spins = 0; spins < kSpinCap; ++spins) {
    const u64 a = __hip_atomic_load((gu64*)p0, RLX_AGENT);
    if (wall(a == tag)) break;
    __builtin_amdgcn_s_sleep(2);
  }
  __builtin_amdgcn_fence(__ATOMIC_ACQUIRE, "wavefront");
}
DI void set_flag(char* p, u64 tag) { __hip_atomic_store((gu64*)p, tag, RLX_AGENT); }
DI u64 make_tag(u64 e) {
  const u64 u = ((u64)rfl((unsigned)(e >> 32)) << 32) | rfl((unsigned)e);
  return u * 0x9E3779B97F4A7C15ull + 0xD1B54A32D192ED03ull;
}

DI void glds4(unsigned v0, unsigned v1, unsigned v2, unsigned v3, const void* sbase, unsigned m0v) {
  unsigned sv;
  asm volatile("s_mov_b32 %0, m0\n\ts_mov_b32 m0, %6\n\ts_nop 0\n\t"
               "global_load_lds_dwordx4 %1, %5\n\t"
               "global_load_lds_dwordx4 %2, %5 offset:1024\n\t"
               "global_load_lds_dwordx4 %3, %5 offset:2048\n\t"
               "global_load_lds_dwordx4 %4, %5 offset:3072\n\t"
               "s_mov_b32 m0, %0"
               : "=&s"(sv) : "v"(v0), "v"(v1), "v"(v2), "v"(v3), "s"(sbase), "s"(m0v) : "memory");
}
DI void glds4_sc1(unsigned v0, unsigned v1, unsigned v2, unsigned v3, const void* sbase, unsigned m0v) {
  unsigned sv;
  asm volatile("s_mov_b32 %0, m0\n\ts_mov_b32 m0, %6\n\ts_nop 0\n\t"
               "global_load_lds_dwordx4 %1, %5 sc1\n\t"
               "global_load_lds_dwordx4 %2, %5 offset:1024 sc1\n\t"
               "global_load_lds_dwordx4 %3, %5 offset:2048 sc1\n\t"
               "global_load_lds_dwordx4 %4, %5 offset:3072 sc1\n\t"
               "s_mov_b32 m0, %0"
               : "=&s"(sv) : "v"(v0), "v"(v1), "v"(v2), "v"(v3), "s"(sbase), "s"(m0v) : "memory");
}
DI void glds1_sc1(unsigned v0, const void* sbase, unsigned m0v) {
  unsigned sv;
  asm volatile("s_mov_b32 %0, m0\n\ts_mov_b32 m0, %3\n\ts_nop 0\n\tglobal_load_lds_dwordx4 %1, %2 sc1\n\ts_mov_b32 m0, %0"
               : "=&s"(sv) : "v"(v0), "s"(sbase), "s"(m0v) : "memory");
}
DI void touch(unsigned voff, const void* sbase, unsigned m0v) {
  unsigned sv;
  asm volatile("s_mov_b32 %0, m0\n\ts_mov_b32 m0, %3\n\ts_nop 0\n\tglobal_load_lds_dword %1, %2\n\ts_mov_b32 m0, %0"
               : "=&s"(sv) : "v"(voff), "s"(sbase), "s"(m0v) : "memory");
}
DI void touch_sc1(unsigned voff, const void* sbase, unsigned m0v) {
  unsigned sv;
  asm volatile("s_mov_b32 %0, m0\n\ts_mov_b32 m0, %3\n\ts_nop 0\n\tglobal_load_lds_dword %1, %2 sc1\n\ts_mov_b32 m0, %0"
               : "=&s"(sv) : "v"(voff), "s"(sbase), "s"(m0v) : "memory");
}
DI void st_gran(char* p, unsigned val, u64 tag) { __hip_atomic_store((gu64*)p, ((u64)(unsigned)tag << 32) | val, RLX_AGENT); }
#define SGB(mask, n) __builtin_amdgcn_sched_group_barrier(mask, n, 0)

constexpr int WR = 65536;
constexpr int SCR = 147456;
constexpr int GWS = WR + 65536, GWA = WR + 16384;
constexpr int SCR_Q = WR;
constexpr int SCR_STAT = 159744, SCR_CS = SCR_STAT + 1024, SCR_CG = SCR_STAT + 1536, SCR_TAG = SCR_STAT + 2304, SCR_KN = SCR_STAT + 2320, SCR_M = SCR_STAT + 2560;
constexpr int SCR_DUMMY = 163584;
constexpr int AT_CH = 2 * (int)KVGS;
constexpr int AT_A = 0, AT_B = 86016;
constexpr int AT_6 = 142336, AT_7 = 120576;
constexpr int AT_RP = AT_7;
constexpr int AT_EX = AT_B;
constexpr int AT_SY = 107520;
constexpr int D_PS = 102400;
constexpr int D_SY = AT_6 + 5120 + 1344;

template <int CTRL> DI float dpp(float x) { return u2f(__builtin_amdgcn_update_dpp(0u, f2u(x), CTRL, 0xf, 0xf, true)); }
DI float sum8(float x) { x += dpp<0xB1>(x); x += dpp<0x4E>(x); x += dpp<0x141>(x); return x; }
DI float sum16(float x) { x = sum8(x); x += dpp<0x140>(x); return x; }
DI float max16(float x) { x = fmaxf(x, dpp<0xB1>(x)); x = fmaxf(x, dpp<0x4E>(x)); x = fmaxf(x, dpp<0x141>(x)); x = fmaxf(x, dpp<0x140>(x)); return x; }
template <int CTRL> DI unsigned dppu(unsigned x) { return __builtin_amdgcn_update_dpp(0u, x, CTRL, 0xf, 0xf, true); }
DI unsigned umax16(unsigned x) { x = max(x, dppu<0xB1>(x)); x = max(x, dppu<0x4E>(x)); x = max(x, dppu<0x141>(x)); x = max(x, dppu<0x140>(x)); return x; }
DI float sumx16(float x) { auto r = __builtin_amdgcn_permlane16_swap(f2u(x), f2u(x), false, false); return u2f(r[0]) + u2f(r[1]); }
DI float sumx32(float x) { auto r = __builtin_amdgcn_permlane32_swap(f2u(x), f2u(x), false, false); return adds(u2f(r[0]), u2f(r[1])); }
DI float maxx16(float x) { auto r = __builtin_amdgcn_permlane16_swap(f2u(x), f2u(x), false, false); return fmaxf(u2f(r[0]), u2f(r[1])); }
DI float maxx32(float x) { auto r = __builtin_amdgcn_permlane32_swap(f2u(x), f2u(x), false, false); return fmaxf(u2f(r[0]), u2f(r[1])); }
DI u32x4 bld16_sc1(__amdgpu_buffer_rsrc_t rs, unsigned off) { return __builtin_amdgcn_raw_buffer_load_b128(rs, off, 0, 16); }
DI u32x2 bld8_sc1(__amdgpu_buffer_rsrc_t rs, unsigned off) { return __builtin_amdgcn_raw_buffer_load_b64(rs, off, 0, 16); }
DI float sq2(unsigned u, float c) { const bf16x2_t v = __builtin_bit_cast(bf16x2_t, u); return __builtin_amdgcn_fdot2_f32_bf16(v, v, c, false); }
DI unsigned read_sy(lds_ptr p) { return *(volatile LAS unsigned*)p; }
DI void bar_() { asm volatile("s_barrier" ::: "memory"); }

struct Pend { u32x4 v[4][2]; unsigned off[4]; const char* base; unsigned str; };
template <bool SWAP>
DI void qkv_compute(lds_ptr lds, int cw, int lane, int rb, int cb, char* ws, Pend& pd TARGS) {
  const int wm = cw >> 1, wn = cw & 1, r = lane & 15, g = lane >> 4;
  lds_ptr xa[2], wa[4];
#pragma unroll
  for (int ks = 0; ks < 2; ++ks) xa[ks] = lds + (wm * 64 + r) * 128 + (((4 * ks + g) ^ ((r >> 1) & 7)) * 16);
  {
    const int q = r >> 2, p = r & 3, f = q | ((g & 1) << 2);
#pragma unroll
    for (int nt = 0; nt < 4; ++nt) wa[nt] = lds + WR + (8 * g + q) * 256 + (((wn * 4 + nt) ^ f) * 32) + p * 8;
  }
  f32x4 acc[4][4];
#pragma unroll
  for (int i = 0; i < 4; ++i)
#pragma unroll
    for (int j = 0; j < 4; ++j) acc[i][j] = f32x4{0.f, 0.f, 0.f, 0.f};
  bf16x8 xf[2][4], wf[2][4];
#define Q_READ(B, SO, KS)                                                                                           \
  do {                                                                                                              \
    _Pragma("unroll") for (int i = 0; i < 4; ++i) {                                                                 \
      wf[B][i] = cat8(ldstr(wa[i] + (((SO) + 49152) & 65535) + (KS) * 8192), ldstr(wa[i] + (((SO) + 49152) & 65535) + (KS) * 8192 + 1024)); \
      xf[B][i] = lds128(xa[KS] + (SO) + i * 2048);                                                                  \
    }                                                                                                               \
  } while (0)
#define Q_MMA(B)                                                                                                    \
  do {                                                                                                              \
    _Pragma("unroll") for (int nt = 0; nt < 4; ++nt)                                                                \
      _Pragma("unroll") for (int mt = 0; mt < 4; ++mt)                                                              \
        acc[nt][mt] = SWAP ? MFMA16(wf[B][nt], xf[B][mt], acc[nt][mt]) : MFMA16(xf[B][mt], wf[B][nt], acc[nt][mt]); \
  } while (0)
#define Q_MIX() do { _Pragma("unroll") for (int i = 0; i < 12; ++i) { SGB(0x8, 1); SGB(0x100, 1); } SGB(0x8, 4); } while (0)
  WVB(1);
  Q_READ(0, 0, 0);
  int so = 0;
#pragma unroll 1
  for (int st = 0; st < 9; ++st) {
    SBAR();
    Q_READ(1, so, 1); Q_MMA(0); Q_MIX();
    SBAR();
    WLB();
    so = (so + 16384) & 65535;
    SBAR();
    Q_READ(0, so, 0); Q_MMA(1); Q_MIX();
  }
  SBAR();
  Q_READ(1, 16384, 1); Q_MMA(0); Q_MIX();
  SBAR();
  Q_MMA(1);
#undef Q_READ
#undef Q_MMA
#undef Q_MIX
  const LAS f32x2* stat = (const LAS f32x2*)(lds + SCR_STAT);
  const LAS float* csum = (const LAS float*)(lds + SCR_CS);
  const int kind = cb / 5, fb = (cb - kind * 5) * 128 + wn * 64;
  if (SWAP) {
    f32x2 st4[4];
    const float qs = kind == 0 ? 0.22360679774997896f * 1.4426950408889634f : 1.0f;
#pragma unroll
    for (int mt = 0; mt < 4; ++mt) { st4[mt] = stat[wm * 64 + mt * 16 + r]; st4[mt][1] *= qs; }
    const char* img = kind == 0 ? ws + WS_Q + (size_t)rb * QGS : ws + WS_KV + (size_t)rb * KVGS;
    const unsigned hs = kind == 0 ? (unsigned)QHS : (unsigned)KVHS;
    const unsigned rowoff = kind == 0 ? r * 16 : ((r >> 3) * 16 + (r & 7)) * 16, mstr = kind == 0 ? 256 : 128;
    float nmax[4]; unsigned kpo[4];
#pragma unroll
    for (int nt = 0; nt < 4; ++nt) {
      const int nl = nt * 16 + 4 * g;
      const f32x4 cs = *(const LAS f32x4*)(csum + wn * 64 + nl);
      const int f0 = fb + nl, head = f0 / 20, sl = (f0 - head * 20) >> 2;
      const unsigned base = head * hs + sl * 1024 + wm * 512 + rowoff;
      nmax[nt] = 0.f; kpo[nt] = (head * 32 + rb * 2 + wm) * 8 + sl;
      unsigned pk[4][2];
#pragma unroll
      for (int mt = 0; mt < 4; ++mt) {
        float v[4];
#pragma unroll
        for (int e = 0; e < 4; ++e) v[e] = (acc[nt][mt][e] - st4[mt][0] * cs[e]) * st4[mt][1];
        pk[mt][0] = cvtpk(v[0], v[1]); pk[mt][1] = cvtpk(v[2], v[3]);
        nmax[nt] = fmaxf(nmax[nt], sq2(pk[mt][1], sq2(pk[mt][0], 0.f)));
      }
      pd.off[nt] = base;
      pd.v[nt][0] = u32x4{pk[0][0], pk[0][1], pk[2][0], pk[2][1]};
      pd.v[nt][1] = u32x4{pk[1][0], pk[1][1], pk[3][0], pk[3][1]};
    }
    pd.base = img; pd.str = mstr;
    if (kind == 1) {
      const bool h0 = r & 1, h1 = r & 2;
#pragma unroll
      for (int nt = 0; nt < 4; ++nt) nmax[nt] = u2f(umax16(f2u(nmax[nt])));
      const float a = h1 ? (h0 ? nmax[3] : nmax[2]) : (h0 ? nmax[1] : nmax[0]);
      const unsigned o = h1 ? (h0 ? kpo[3] : kpo[2]) : (h0 ? kpo[1] : kpo[0]);
      if (r < 4) __hip_atomic_store((GAS float*)(ws + WS_KP) + o, a, RLX_AGENT);
    }
  } else {
    const char* img = ws + WS_KV + (size_t)rb * KVGS + 5120;
    unsigned off[4]; float cs[4];
#pragma unroll
    for (int nt = 0; nt < 4; ++nt) {
      const int nl = nt * 16 + r, f = fb + nl, head = f / 20, d = f - head * 20;
      cs[nt] = csum[wn * 64 + nl];
      off[nt] = head * (unsigned)KVHS + wm * 2816 + d * 64 + ((g ^ ((d >> 2) & 3)) * 16);
    }
#pragma unroll
    for (int j = 0; j < 2; ++j) {
      f32x4 sa[2], sb[2];
#pragma unroll
      for (int i = 0; i < 2; ++i) {
        sa[i] = *(const LAS f32x4*)(stat + wm * 64 + (2 * j + i) * 16 + 4 * g);
        sb[i] = *(const LAS f32x4*)(stat + wm * 64 + (2 * j + i) * 16 + 4 * g + 2);
      }
#pragma unroll
      for (int nt = 0; nt < 4; ++nt) {
        u32x4 o;
#pragma unroll
        for (int i = 0; i < 2; ++i) {
          const f32x4 a = acc[nt][2 * j + i];
          o[2 * i] = cvtpk((a[0] - sa[i][0] * cs[nt]) * sa[i][1], (a[1] - sa[i][2] * cs[nt]) * sa[i][3]);
          o[2 * i + 1] = cvtpk((a[2] - sb[i][0] * cs[nt]) * sb[i][1], (a[3] - sb[i][2] * cs[nt]) * sb[i][3]);
        }
        pd.v[nt][j] = o;
      }
    }
#pragma unroll
    for (int nt = 0; nt < 4; ++nt) pd.off[nt] = off[nt];
    pd.base = img; pd.str = 1408;
    {
      const int t = cw * 64 + lane, h0 = ((cb - 10) * 128) / 20, h1 = ((cb - 9) * 128) / 20, hd = h0 + (t >> 5);
      const unsigned c = (t & 4) ? 0u : 0x3f803f80u;
      if (hd < h1) st16_sc1(img, hd * (unsigned)KVHS + ((t >> 3) & 3) * 1408 + 1280 + (t & 7) * 16, u32x4{c, c, c, c});
    }
  }
}

DI void gu_compute(lds_ptr lds, int cw, int lane, int rb, int fbk, char* ws, const Pend& pd, bool has_pd TARGS) {
  const int wm = cw >> 1, wn = cw & 1, r = lane & 15, g = lane >> 4;
  lds_ptr xa[2], wa[3];
#pragma unroll
  for (int ks = 0; ks < 2; ++ks) xa[ks] = lds + (wm * 64 + r) * 128 + (((4 * ks + g) ^ ((r >> 1) & 7)) * 16);
#pragma unroll
  for (int nt = 0; nt < 3; ++nt) wa[nt] = lds + (8 * g + (r >> 2)) * 192 + (((wn * 3 + nt) ^ (g & 1)) * 32) + (r & 3) * 8;
  f32x4 acc[6][4];
#pragma unroll
  for (int i = 0; i < 6; ++i)
#pragma unroll
    for (int j = 0; j < 4; ++j) acc[i][j] = f32x4{0.f, 0.f, 0.f, 0.f};
  bf16x8 xf[2][4], wf[2][6];
#define G_READ(B, SO, WO, KS)                                                                                           \
  do {                                                                                                              \
    _Pragma("unroll") for (int i = 0; i < 6; ++i) {                                                                 \
      wf[B][i] = cat8(ldstr(wa[i % 3] + (WO) + (i / 3) * 12288 + (KS) * 6144), ldstr(wa[i % 3] + (WO) + (i / 3) * 12288 + (KS) * 6144 + 768)); \
      if (i < 4) xf[B][i] = lds128(xa[KS] + (SO) + i * 2048);                                                       \
    }                                                                                                               \
  } while (0)
#define G_MMA(B)                                                                                                    \
  do {                                                                                                              \
    _Pragma("unroll") for (int nt = 0; nt < 6; ++nt)                                                                \
      _Pragma("unroll") for (int mt = 0; mt < 4; ++mt) acc[nt][mt] = MFMA16(wf[B][nt], xf[B][mt], acc[nt][mt]);     \
  } while (0)
#define G_MIX() do { _Pragma("unroll") for (int i = 0; i < 16; ++i) { SGB(0x8, 1); SGB(0x100, 1); } SGB(0x8, 8); } while (0)
  int so = 32768, wo = GWS;
  if (has_pd) {
#pragma unroll
    for (int i = 0; i < 4; ++i) { st16_sc1(pd.base, pd.off[i], pd.v[i][0]); st16_sc1(pd.base + pd.str, pd.off[i], pd.v[i][1]); }
  }
  WLB();
  G_READ(0, so, wo, 0);
#pragma unroll 1
  for (int st = 10; st < 19; ++st) {
    SBAR();
    G_READ(1, so, wo, 1); G_MMA(0); G_MIX();
    SBAR();
    if (st == 11) asm volatile("s_waitcnt vmcnt(0)" ::: "memory");
    WLB();
    so = (so + 16384) & 65535;
    wo = wo == GWS ? GWA : wo + 24576;
    SBAR();
    G_READ(0, so, wo, 0); G_MMA(1); G_MIX();
  }
  SBAR();
  G_READ(1, 49152, GWS, 1); G_MMA(0); G_MIX();
  SBAR();
  WLB();
  G_MMA(1);
#undef G_READ
#undef G_MMA
#undef G_MIX
  const LAS f32x2* stat = (const LAS f32x2*)(lds + SCR_STAT);
  const LAS float* csum = (const LAS float*)(lds + SCR_CG);
  f32x2 st4[4];
#pragma unroll
  for (int mt = 0; mt < 4; ++mt) { st4[mt] = stat[wm * 64 + mt * 16 + r]; st4[mt][1] *= st4[mt][1]; }
  const unsigned ho = (((rb * 8 + wm * 4 + (g & 1)) * 192 + fbk * 12 + wn * 6 + (g >> 1)) * 16 + r) * 16;
#pragma unroll
  for (int nt = 0; nt < 3; ++nt) {
    const f32x4 cg = *(const LAS f32x4*)(csum + wn * 48 + nt * 16 + 4 * g), cu = *(const LAS f32x4*)(csum + 96 + wn * 48 + nt * 16 + 4 * g);
    unsigned w[4][2];
#pragma unroll
    for (int mt = 0; mt < 4; ++mt) {
      float hv[4];
#pragma unroll
      for (int e = 0; e < 4; ++e) {
        const float gv = acc[nt][mt][e] - st4[mt][0] * cg[e], uv = acc[3 + nt][mt][e] - st4[mt][0] * cu[e];
        hv[e] = fmaxf(gv, 0.f) * (uv * st4[mt][1]);
      }
      w[mt][0] = cvtpk(hv[0], hv[1]); w[mt][1] = cvtpk(hv[2], hv[3]);
    }
#pragma unroll
    for (int j = 0; j < 2; ++j) {
      auto s0 = __builtin_amdgcn_permlane16_swap(w[2 * j][0], w[2 * j + 1][0], false, false);
      auto s1 = __builtin_amdgcn_permlane16_swap(w[2 * j][1], w[2 * j + 1][1], false, false);
      st16_sc1(ws + WS_H, ho + j * (2 * 192 * 256) + nt * 512, u32x4{s0[0], s1[0], s0[1], s1[1]});
    }
  }
}

#if defined(__HIP_DEVICE_COMPILE__) && NOPK
#define KATTR __attribute__((target("no-packed-fp32-ops")))
#else
#define KATTR
#endif
__global__ __launch_bounds__(512, 2) KATTR void fused_block(const char* __restrict__ x, const char* __restrict__ wqkv, const char* __restrict__ wo,
                                                      const char* __restrict__ wg, const char* __restrict__ wu, const char* __restrict__ wd,
                                                      char* __restrict__ ws, char* __restrict__ out) {
  __shared__ __attribute__((aligned(16))) char smem[LDS_BYTES];
  lds_ptr lds = (lds_ptr)smem;
  const unsigned ldsb = (unsigned)(size_t)lds;
  const int lane = threadIdx.x & 63, wid = rfl(threadIdx.x >> 6), bid = blockIdx.x;
  const int xcd = bid & 7, idx = bid >> 3;
  const int tq = (xcd & 1) * 30 + idx, cb = tq >> 2, rb = (xcd >> 1) * 4 + (tq & 3);
  const int fbk = (xcd & 1) * 8 + (idx >> 2);
  const int head = xcd * 4 + (idx >> 3), qb = idx & 7;
  const bool has_qkv = idx < 30;
  const int rot_g = ((tq & 3) * 5 + 1) / 2 + (xcd >> 1);
  const int sg0 = rot_g >= 10 ? rot_g - 10 : rot_g, sq0 = (rot_g + cb) % 10;
#define NEXT10(k) ((k) == 9 ? 0 : (k) + 1)
  const int r32 = lane & 31, hh = lane >> 5;
  const char* kvg = ws + WS_KV + (size_t)head * KVHS;
  const unsigned vl = lane * 16;
  u64 tag = 0;
  bf16x8 qa[2]; s16x4 qv[2][2]; float mrow[2];
#define AT_ISSUE(C, SLOT)                                                                                      \
  do {                                                                                                         \
    const char* src_ = kvg + (size_t)(C) * AT_CH;                                                              \
    const unsigned dst_ = ldsb + (SLOT);                                                                       \
    unsigned sv_;                                                                                              \
    asm volatile("s_mov_b32 %0, m0\n\ts_mov_b32 m0, %5\n\ts_nop 0\n\t"                                         \
                 "global_load_lds_dwordx4 %1, %2 sc1\n\tglobal_load_lds_dwordx4 %1, %2 offset:1024 sc1\n\t"    \
                 "global_load_lds_dwordx4 %1, %2 offset:2048 sc1\n\tglobal_load_lds_dwordx4 %1, %2 offset:3072 sc1\n\t" \
                 "s_mov_b32 m0, %6\n\ts_nop 0\n\tglobal_load_lds_dwordx4 %1, %3 sc1\n\t"                       \
                 "s_mov_b32 m0, %7\n\ts_nop 0\n\tglobal_load_lds_dwordx4 %1, %4 sc1\n\t"                       \
                 "s_mov_b32 m0, %0"                                                                            \
                 : "=&s"(sv_)                                                                                  \
                 : "v"(vl), "s"(src_ + wq * 5120), "s"(src_ + wq * 5120 + 4096), "s"(src_ + 20480), "s"(rfl(dst_ + wq * 5120)), \
                   "s"(rfl(dst_ + wq * 5120 + 4096)), "s"(rfl(dst_ + 20480))                                           \
                 : "memory");                                                                                  \
  } while (0)

#define AT_ISSUE2(C, SLOT)                                                                                     \
  do {                                                                                                         \
    const char* src_ = kvg + (size_t)(C) * AT_CH + wid * 10240;                                                \
    const unsigned dst_ = ldsb + (SLOT) + wid * 10240;                                                         \
    glds4_sc1(vl, vl, vl, vl, src_, rfl(dst_));                                                                \
    glds4_sc1(vl, vl, vl, vl, src_ + 4096, rfl(dst_ + 4096));                                                  \
    glds1_sc1(vl, src_ + 8192, rfl(dst_ + 8192)); glds1_sc1(vl, src_ + 9216, rfl(dst_ + 9216));                \
  } while (0)
  if (wid == 7) {
    const char* pc;
    asm volatile("s_getpc_b64 %0" : "=s"(pc));
    const int ln = idx * ((CODE_LINES + 31) / 32) + (lane < (CODE_LINES + 31) / 32 ? lane : 0);
    touch((ln < CODE_LINES ? ln : 0) * 128, (const char*)((size_t)pc & ~(size_t)255), ldsb + SCR_DUMMY);
  }
  u32x4 rv0, rv1; u32x2 gep;
  asm volatile("global_load_dwordx4 %0, %3, %5\n\tglobal_load_dwordx4 %1, %4, %5 offset:1024\n\tglobal_load_dwordx2 %2, %6, %7 sc1"
               : "=&v"(rv0), "=&v"(rv1), "=&v"(gep) : "v"(vl), "v"(vl & 255), "s"(x + (size_t)(bid * 8 + wid) * 1280), "v"(0u), "s"(ws + WS_EP) : "memory");
  unsigned vx[4], vw[4];
#pragma unroll
  for (int i = 0; i < 4; ++i) {
    const int row = 8 * i + (lane >> 3), ch = (lane & 7) ^ ((4 * i + (lane >> 4)) & 7);
    vx[i] = row * 1280 + ch * 16 - 1024 * i;
    const int k = 4 * i + (lane >> 4), f = (lane >> 4) | ((i >> 1) << 2), cw = (lane & 15) ^ (f << 1);
    vw[i] = k * 3840 + cw * 16 - 1024 * i;
  }
  if (has_qkv) {
    if (wid < 4) {
      const char* p = x + (size_t)(rb * 128 + wid * 32) * 1280;
      glds4(vx[0], vx[1], vx[2], vx[3], p + sq0 * 128, ldsb + wid * 4096);
    } else {
      const char* p = wqkv + cb * 256 + (size_t)(wid - 4) * 16 * 3840;
      glds4(vw[0], vw[1], vw[2], vw[3], p + (size_t)sq0 * (64 * 3840), ldsb + WR + 49152 + (wid - 4) * 4096);
    }
  }
  {
    if (!has_qkv) asm volatile("s_waitcnt vmcnt(0)" ::: "memory");
    asm volatile("s_waitcnt vmcnt(4)" : "+v"(rv0), "+v"(rv1), "+v"(gep)::"memory");
    tag = make_tag(((u64)gep[1] << 32) | gep[0]);
    float s0 = 0.f, s1 = 0.f;
#pragma unroll
    for (int e = 0; e < 4; ++e) { s0 += bflo(rv0[e]) + bfhi(rv0[e]); s1 += bflo(rv1[e]) + bfhi(rv1[e]); }
    const float mean = sumx32(sumx16(sum16(lane < 16 ? s0 + s1 : s0))) * (1.0f / D);
    float q0 = 0.f, q1 = 0.f;
#pragma unroll
    for (int e = 0; e < 4; ++e) {
      const float a = bflo(rv0[e]) - mean, b = bfhi(rv0[e]) - mean, c = bflo(rv1[e]) - mean, d = bfhi(rv1[e]) - mean;
      q0 += a * a + b * b; q1 += c * c + d * d;
    }
    const float den = sqrtf(bfr(bfr(sumx32(sumx16(sum16(lane < 16 ? q0 + q1 : q0))) * (1.0f / D)) + 1e-5f));
    if (lane == 0) st_gran(ws + WS_ST + (size_t)(bid * 8 + wid) * 8, cvtpk(mean, den), tag);
  }
  if (wid < 2) {
    __builtin_amdgcn_s_setprio(3);
    unsigned vg[6];
#pragma unroll
    for (int j = 0; j < 6; ++j) {
      const int u = j * 64 + lane, k = u / 12, pos = u - k * 12;
      vg[j] = k * 3072 + (pos ^ (((k >> 3) & 1) * 2)) * 16 - 1024 * (j & 3);
    }
    const char* xb = x + (size_t)(rb * 128 + wid * 64) * 1280;
    const unsigned lx = ldsb + wid * 8192;
#define LX_ISSUE(ST, SO) do { glds4(vx[0], vx[1], vx[2], vx[3], xb + (ST) * 128, lx + (SO)); glds4(vx[0], vx[1], vx[2], vx[3], xb + (ST) * 128 + 32 * 1280, lx + 4096 + (SO)); } while (0)
    if (has_qkv) {
      const char* wb = wqkv + cb * 256 + (size_t)wid * 32 * 3840;
#define LQ_X(ST) do { kx = NEXT10(kx); LX_ISSUE(kx, ((ST) & 3) * 16384); } while (0)
#define LQ_W(ST)                                                                                                \
  do {                                                                                                          \
    kw = NEXT10(kw);                                                                                            \
    glds4(vw[0], vw[1], vw[2], vw[3], wb + (size_t)kw * (64 * 3840), lx + WR + (((ST) + 3) & 3) * 16384);           \
    glds4(vw[0], vw[1], vw[2], vw[3], wb + (size_t)kw * (64 * 3840) + 16 * 3840, lx + WR + 4096 + (((ST) + 3) & 3) * 16384); \
  } while (0)
      int kx = sq0, kw = sq0;
      LQ_X(1); LQ_W(1);
      asm volatile("" : "+v"(vg[0]), "+v"(vg[1]), "+v"(vg[2]), "+v"(vg[3]), "+v"(vg[4]), "+v"(vg[5]));
      WVB(17);
      LQ_X(2); LQ_W(2);
      WVB(16);
      LQ_X(3); LQ_W(3); LQ_X(4);
      WVB(24);
      LQ_W(4); LQ_X(5); LQ_W(5);
#pragma unroll 1
      for (int k = 3; k < 7; ++k) {
        WVB(32);
        LQ_X(k + 3); LQ_W(k + 3);
      }
#undef LQ_X
#undef LQ_W
    }
    const char* wb = (wid ? wu : wg) + fbk * 192;
    const unsigned lw = ldsb + wid * 12288;
#define LG_W(J, WO)                                                                                             \
  do {                                                                                                          \
    glds4(vg[0], vg[1], vg[2], vg[3], wb + (size_t)(J) * (64 * 3072), lw + (WO));                               \
    glds2(vg[4], vg[5], wb + (size_t)(J) * (64 * 3072), lw + 4096 + (WO));                                      \
    glds4(vg[0], vg[1], vg[2], vg[3], wb + (size_t)(J) * (64 * 3072) + 32 * 3072, lw + 6144 + (WO));            \
    glds2(vg[4], vg[5], wb + (size_t)(J) * (64 * 3072) + 32 * 3072, lw + 6144 + 4096 + (WO));                   \
  } while (0)
    int kw = sg0, kx = sg0;
    LG_W(kw, GWS);
    if (has_qkv) { WVB(44); WVB(28); }
    kw = NEXT10(kw); LG_W(kw, GWA);
    if (has_qkv) WVB(24);
    LX_ISSUE(kx, 32768); kx = NEXT10(kx);
    LX_ISSUE(kx, 49152); kx = NEXT10(kx);
    kw = NEXT10(kw); LG_W(kw, GWA + 24576);
    LX_ISSUE(kx, 0); kx = NEXT10(kx);
    WVB(28);
    LX_ISSUE(kx, 16384); kx = NEXT10(kx);
    int wo = GWS;
#pragma unroll 1
    for (int j = 3; j < 9; ++j) {
      WVB(28);
      kw = NEXT10(kw); LG_W(kw, wo);
      LX_ISSUE(kx, ((j + 3) & 3) * 16384); kx = NEXT10(kx);
      wo = wo == GWS ? GWA : wo + 24576;
    }
    WVB(28);
    kw = NEXT10(kw); LG_W(kw, GWS);
#undef LG_W
#undef LX_ISSUE
    WVB(20);
    {
      const char* qg = ws + WS_Q + (size_t)head * QHS + (size_t)(qb * 2 + wid) * QGS;
      glds4_sc1(vl, vl, vl, vl, qg, ldsb + SCR_Q + wid * 5120);
      glds1_sc1(vl, qg + 4096, ldsb + SCR_Q + wid * 5120 + 4096);
    }
    AT_ISSUE2(0, AT_A);
    AT_ISSUE2(1, AT_B);
    WVB(20);
    glds1_sc1(vl, kvg + (size_t)wid * AT_CH + 20480, rfl(ldsb + (wid ? AT_B : AT_A) + 20480));
  } else if (wid < 4) {
    const int hw = wid - 2, ht = hw * 64 + lane;
    const __amdgpu_buffer_rsrc_t rs = __builtin_amdgcn_make_buffer_rsrc(ws, 0, (int)WS_DBG, 0x00020000);
    __builtin_amdgcn_s_setprio(3);
#define HBAR() do { if (has_qkv) { SBAR(); BAR(); SBAR(); } } while (0)
#define PIN5(V) asm volatile("" : "+v"(V[0]), "+v"(V[1]), "+v"(V[2]), "+v"(V[3]), "+v"(V[4]))
    const bf16x8 ones = {0x3f80, 0x3f80, 0x3f80, 0x3f80, 0x3f80, 0x3f80, 0x3f80, 0x3f80};
    const int r = lane & 15, g = lane >> 4;
    const unsigned t32 = (unsigned)tag;
    char* csd = ws + WS_CS + (size_t)((cb * 16 + sq0) * 128 + hw * 64 + (r >> 2) * 16 + 4 * g + (r & 3)) * 8;
    char* cgd = ws + WS_CG + (size_t)((fbk * 16 + sg0) * 192 + hw * 96 + (r >> 2) * 16 + 4 * g + (r & 3)) * 8;
    unsigned sto = (unsigned)WS_ST + (rb * 128 + ht) * 8;
    unsigned cso = (unsigned)WS_CS + ((has_qkv ? cb : 0) * 16 + hh) * 1024 + hw * 512 + r32 * 16;
    unsigned cgo = (unsigned)WS_CG + fbk * 16 * 1536 + hw * 768 + (lane < 48 ? lane : 0) * 16;
    unsigned kpo = (unsigned)WS_KP + (head * 32 + r32) * 32 + hh * 16;
    const int c0 = (head * 20) >> 7, c1 = (head * 20 + 19) >> 7;
    const char* fkv = ws + WS_FQ + ((5 + (g >> 1) * 5 + ((g & 1) ? c1 : c0)) * 16 + r) * 8;
    const char* fq = ws + WS_FQ + (((lane & 1) ? c1 : c0) * 16 + qb * 2 + hw) * 8;
    lds_ptr wqa[4], wga[2];
#pragma unroll
    for (int nt = 0; nt < 4; ++nt) wqa[nt] = lds + WR + 49152 + (8 * g + (r >> 2)) * 256 + (((hw * 4 + nt) ^ ((r >> 2) | ((g & 1) << 2))) * 32) + (r & 3) * 8;
#pragma unroll
    for (int i = 0; i < 2; ++i) wga[i] = lds + GWS + hw * 12288 + (8 * g + (r >> 2)) * 192 + ((i ^ (g & 1)) * 32) + (r & 3) * 8;
    asm volatile("" : "+v"(csd), "+v"(cgd), "+v"(sto), "+v"(cso), "+v"(cgo), "+v"(kpo), "+v"(fkv), "+v"(fq), "+v"(wqa[0]), "+v"(wqa[1]), "+v"(wqa[2]), "+v"(wqa[3]),
                 "+v"(wga[0]), "+v"(wga[1]));
    if (has_qkv) {
      SBAR(); WVB(1); SBAR();
      f32x4 cs[4];
#pragma unroll
      for (int nt = 0; nt < 4; ++nt) {
        cs[nt] = MFMA16(cat8(ldstr(wqa[nt]), ldstr(wqa[nt] + 1024)), ones, (f32x4{0.f, 0.f, 0.f, 0.f}));
        cs[nt] = MFMA16(cat8(ldstr(wqa[nt] + 8192), ldstr(wqa[nt] + 9216)), ones, cs[nt]);
      }
      const f32x4 v = (r & 8) ? ((r & 4) ? cs[3] : cs[2]) : ((r & 4) ? cs[1] : cs[0]);
      st_gran(csd, f2u((r & 2) ? ((r & 1) ? v[3] : v[2]) : ((r & 1) ? v[1] : v[0])), tag);
      SBAR(); WLB(); SBAR();
      BAR(); BAR(); BAR(); BAR();
    }
    {
      u32x2 gst = bld8_sc1(rs, sto);
      u32x4 gc[5];
      if (has_qkv) {
#pragma unroll
        for (int i = 0; i < 5; ++i) gc[i] = bld16_sc1(rs, cso + i * 2048);
        BAR(); BAR();
      }
      _Pragma("unroll 1") for (unsigned spins = 0; spins < kSpinCap; ++spins) {
        unsigned bad = gst[1] ^ t32;
        if (has_qkv) {
#pragma unroll
          for (int i = 0; i < 5; ++i) bad |= (gc[i][1] ^ t32) | (gc[i][3] ^ t32);
        }
        if (wall(bad == 0u)) break;
        __builtin_amdgcn_s_sleep(2);
        asm volatile("" ::: "memory");
        gst = bld8_sc1(rs, sto);
        if (has_qkv) {
#pragma unroll
          for (int i = 0; i < 5; ++i) gc[i] = bld16_sc1(rs, cso + i * 2048);
        }
      }
      HBAR();
      *(LAS f32x2*)(lds + SCR_STAT + ht * 8) = f32x2{bflo(gst[0]), 1.0f / bfhi(gst[0])};
      if (has_qkv) {
        float a = 0.f, b = 0.f;
#pragma unroll
        for (int i = 0; i < 5; ++i) { a = adds(a, u2f(gc[i][0])); b = adds(b, u2f(gc[i][2])); }
        a = sumx32(a); b = sumx32(b);
        if (lane < 32) *(LAS f32x2*)(lds + SCR_CS + hw * 256 + lane * 8) = f32x2{a, b};
        WLB();
      }
    }
    BAR();
    {
      f32x4 cs[6];
#pragma unroll
      for (int nt = 0; nt < 6; ++nt) {
        const lds_ptr wa = wga[nt & 1] + (nt >> 1) * 64;
        cs[nt] = MFMA16(cat8(ldstr(wa), ldstr(wa + 768)), ones, (f32x4{0.f, 0.f, 0.f, 0.f}));
        cs[nt] = MFMA16(cat8(ldstr(wa + 6144), ldstr(wa + 6912)), ones, cs[nt]);
      }
      asm volatile("" : "+v"(cs[0]), "+v"(cs[1]), "+v"(cs[2]), "+v"(cs[3]), "+v"(cs[4]), "+v"(cs[5]));
      SBAR(); WLB(); SBAR();
      const f32x4 v = (r & 8) ? ((r & 4) ? cs[3] : cs[2]) : ((r & 4) ? cs[1] : cs[0]), w = (r & 4) ? cs[5] : cs[4];
      st_gran(cgd, f2u((r & 2) ? ((r & 1) ? v[3] : v[2]) : ((r & 1) ? v[1] : v[0])), tag);
      if (r < 8) st_gran(cgd + 512, f2u((r & 2) ? ((r & 1) ? w[3] : w[2]) : ((r & 1) ? w[1] : w[0])), tag);
    }
    BAR();
    if (ht == 0 && has_qkv) set_flag(ws + WS_FQ + (cb * 16 + rb) * 8, tag);
    BAR();
    {
      u32x4 gc[10];
#pragma unroll
      for (int i = 0; i < 10; ++i) gc[i] = bld16_sc1(rs, cgo + i * 1536);
      BAR(); BAR();
      _Pragma("unroll 1") for (unsigned spins = 0; spins < kSpinCap; ++spins) {
        unsigned bad = 0u;
#pragma unroll
        for (int i = 0; i < 10; ++i) bad |= (gc[i][1] ^ t32) | (gc[i][3] ^ t32);
        if (wall(bad == 0u)) break;
        __builtin_amdgcn_s_sleep(2);
        asm volatile("" ::: "memory");
#pragma unroll
        for (int i = 0; i < 10; ++i) gc[i] = bld16_sc1(rs, cgo + i * 1536);
      }
      SBAR(); BAR(); SBAR();
      const u64 f0 = __hip_atomic_load((gu64*)fkv, RLX_AGENT), f1 = __hip_atomic_load((gu64*)fq, RLX_AGENT);
      float a = 0.f, b = 0.f;
#pragma unroll
      for (int i = 0; i < 10; ++i) { a = adds(a, u2f(gc[i][0])); b = adds(b, u2f(gc[i][2])); }
      if (lane < 48) *(LAS f32x2*)(lds + SCR_CG + hw * 384 + lane * 8) = f32x2{a, b};
      SBAR(); BAR(); SBAR();
      if (!wall(f0 == tag && f1 == tag)) wait_tag(fkv, fq, tag);
      __builtin_amdgcn_fence(__ATOMIC_ACQUIRE, "wavefront");
    }
    {
      const u32x4 kp = bld16_sc1(rs, kpo);
      BAR();
      const float ks = hh ? u2f(kp[0]) : adds(adds(u2f(kp[0]), u2f(kp[1])), adds(u2f(kp[2]), u2f(kp[3])));
      const float kn2 = maxx16(max16(sumx32(ks)));
      if (ht == 0) *(LAS float*)(lds + SCR_KN) = kn2;
      WLB();
    }
  } else {
    const int cw = wid - 4;
    __builtin_amdgcn_s_setprio(2);
    Pend pd = {};
    pd.base = ws;
    if (has_qkv) {
      if (cb < 10) qkv_compute<true>(lds, cw, lane, rb, cb, ws, pd TPASS);
      else qkv_compute<false>(lds, cw, lane, rb, cb, ws, pd TPASS);
    }
    gu_compute(lds, cw, lane, rb, fbk, ws, pd, has_qkv TPASS);
    __builtin_amdgcn_s_setprio(0);
  }
  {
    const lds_ptr qp = lds + SCR_Q + ((wid >> 1) & 1) * 5120 + (wid & 1) * 512 + r32 * 16;
    const u32x4 l0 = __builtin_bit_cast(u32x4, lds128(qp + hh * 2048)), l1 = __builtin_bit_cast(u32x4, lds128(qp + hh * 2048 + 1024));
    const u32x4 qbb = __builtin_bit_cast(u32x4, lds128(qp + 4096));
#pragma unroll
    for (int a = 0; a < 2; ++a) {
      qa[a] = __builtin_bit_cast(bf16x8, u32x4{l0[2 * a], l0[2 * a + 1], l1[2 * a], l1[2 * a + 1]});
#pragma unroll
      for (int j = 0; j < 2; ++j) { u32x2 u = {hh == j ? qbb[2 * a] : 0u, hh == j ? qbb[2 * a + 1] : 0u}; qv[a][j] = __builtin_bit_cast(s16x4, u); }
    }
    LAS f32x2* mp = (LAS f32x2*)(lds + SCR_M + (wid & 3) * 256 + r32 * 8);
    if (wid < 4) {
      const float kn2 = *(const LAS float*)(lds + SCR_KN);
#pragma unroll
      for (int a = 0; a < 2; ++a) {
        const unsigned w[5] = {hh ? qbb[2 * a + 1] : qbb[2 * a], l0[2 * a], l0[2 * a + 1], l1[2 * a], l1[2 * a + 1]};
        float ss = 0.f;
#pragma unroll
        for (int e = 0; e < 5; ++e) { ss = fmas(bflo(w[e]), bflo(w[e]), ss); ss = fmas(bfhi(w[e]), bfhi(w[e]), ss); }
        mrow[a] = __builtin_amdgcn_sqrtf(fmas(sumx32(ss), kn2 * 1.04f, 0.f));
      }
      *mp = f32x2{mrow[0], mrow[1]};
      WVB(0);
      __builtin_amdgcn_s_setprio(0);
    } else {
      const f32x2 m = *mp;
      mrow[0] = m[0]; mrow[1] = m[1];
    }
  }

  const int kh = wid >> 2;
#define FRESH()                                                                                                \
  int lane_ = lane, bid_ = bid, wq = wid & 3; char* wz = ws;                                                   \
  asm volatile("" : "+v"(lane_));                                                                              \
  asm volatile("" : "+s"(bid_), "+s"(wz), "+s"(wq));                                                           \
  const int xc = bid_ & 7, ix = bid_ >> 3, mb = (xc >> 1) * 8 + (ix & 7), nb = (xc & 1) * 4 + (ix >> 3);       \
  const unsigned vl = lane_ * 16;                                                                              \
  const char* kvg = wz + WS_KV + (size_t)(xc * 4 + (ix >> 3)) * KVHS;                                          \
  (void)vl; (void)mb; (void)nb; (void)kvg
#define FINISH() return
  {
    const int dsel = r32 <= 20 ? r32 : 21;
    const int koff = kh * (int)KVGS + hh * 2048 + r32 * 16, kboff = kh * (int)KVGS + 4096 + lane * 16, vsw = r32 < 28 ? (dsel >> 2) & 3 : 3;
    const int voff0 = kh * (int)KVGS + 5120 + dsel * 64 + ((hh ^ vsw) * 16), voff1 = voff0 ^ 32;
    for (int pass = 0;; ++pass) {
      f32x16 negm[2], sc[2], oacc[2];
#pragma unroll
      for (int a = 0; a < 2; ++a)
#pragma unroll
        for (int e = 0; e < 16; ++e) { negm[a][e] = -mrow[a]; oacc[a][e] = 0.f; }
      asm volatile("" : "+v"(negm[0]), "+v"(negm[1]));
      u32x4 pw[2][2] = {};
      bf16x8 ka, kb, vf[2] = {};
      int sl_cur = AT_A, sl_next = AT_B;
      unsigned hook = wid < 4 ? 0x0fu : 0x5au;
      u64 tbl = (u64)(AT_CH >> 8) | (u64)(2 * AT_CH >> 8) << 10 | (u64)(3 * AT_CH >> 8) << 20 | (u64)(AT_A >> 8) << 30 | (u64)(AT_6 >> 8) << 40 | (u64)(AT_7 >> 8) << 50;
      asm volatile("" : "+s"(hook), "+s"(tbl));
#define LDKA(P, t) cat8(lds64((P) + ((t) >> 1) * 512 + ((t) & 1) * 8), lds64((P) + 1024 + ((t) >> 1) * 512 + ((t) & 1) * 8))
      ka = LDKA(lds + koff + AT_A, 0);
      kb = lds128(lds + kboff + AT_A);
#define KBH(t) (((t) & 1) ? __builtin_shufflevector(kb, kb, 4, 5, 6, 7) : __builtin_shufflevector(kb, kb, 0, 1, 2, 3))
      sc[0] = MFMA32(ka, qa[0], negm[0]);
      sc[0] = MFMA32K8(KBH(0), qv[0][0], sc[0]);
#define EX4(X, i) do { X[i] = __builtin_amdgcn_exp2f(X[i]); X[i + 1] = __builtin_amdgcn_exp2f(X[i + 1]); X[i + 2] = __builtin_amdgcn_exp2f(X[i + 2]); X[i + 3] = __builtin_amdgcn_exp2f(X[i + 3]); } while (0)
#define PK2(P, j, X, i) do { P[j] = cvtpk(X[i], X[i + 1]); P[j + 1] = cvtpk(X[i + 2], X[i + 3]); } while (0)
#define PFR(a, s) __builtin_bit_cast(bf16x8, pw[a][s])
#define UNIT(A, B, TB, LDK, LDV)                                                             \
  do {                                                                                       \
    sc[B] = MFMA32(ka, qa[B], negm[B]); SBAR();                                              \
    EX4(sc[A], 0); PIN(sc[A]); SBAR();                                                       \
    sc[B] = MFMA32K8(KBH(TB), qv[B][(TB) >> 1], sc[B]); SBAR();                              \
    LDK; SBAR();                                                                             \
    EX4(sc[A], 4); PK2(pw[A][0], 0, sc[A], 0); PIN(sc[A]); PIN(pw[A][0]); SBAR();            \
    oacc[B] = MFMA32(vf[0], PFR(B, 0), oacc[B]); SBAR();                                     \
    EX4(sc[A], 8); PK2(pw[A][0], 2, sc[A], 4); PIN(sc[A]); PIN(pw[A][0]); SBAR();            \
    oacc[B] = MFMA32(vf[1], PFR(B, 1), oacc[B]); SBAR();                                     \
    LDV; SBAR();                                                                             \
    EX4(sc[A], 12); PK2(pw[A][1], 0, sc[A], 8); PK2(pw[A][1], 2, sc[A], 12); PIN(pw[A][1]); SBAR(); \
  } while (0)
#define LDV_(t) do { vf[0] = lds128(vp0 + (t) * 1408); vf[1] = lds128(vp1 + (t) * 1408); } while (0)
      for (int c = 0; c < 8; ++c) {
        lds_ptr kp_ = lds + koff + sl_cur, vp0 = lds + voff0 + sl_cur, vp1 = lds + voff1 + sl_cur, kn = lds + koff + sl_next, kbn = lds + kboff + sl_next;
        UNIT(0, 1, 0, ka = LDKA(kp_, 1), LDV_(0));
        UNIT(1, 0, 1, (void)0, (void)0);
        UNIT(0, 1, 1, ka = LDKA(kp_, 2), LDV_(1));
        UNIT(1, 0, 2, (void)0, (void)0);
        UNIT(0, 1, 2, ka = LDKA(kp_, 3), LDV_(2));
        UNIT(1, 0, 3, (void)0, (void)0);
        if (__builtin_expect((hook >> c) & 1, 0)) {
         if (c & 1) WVB(0);
         if (wid >= 4) {
          if (c == 3) __builtin_amdgcn_s_setprio(1);
          else if (c >= 4) {
            FRESH();
            const unsigned lt = (wq * 64 + lane_) * 128, sink = rfl(ldsb + AT_SY + 256);
            if (c == 4) {
              wait_tag(wz + WS_FH + ((xc >> 1) * 64 + lane_) * 8, tag);
              const char* hp = wz + WS_H + (size_t)((xc >> 1) * 32 + ix) * 49152;
              touch_sc1(lt, hp, sink);
              if (wq < 2) touch_sc1(lt, hp + 32768, sink);
              else touch_sc1(lane_ * 1280 + (wq - 2) * 156, x + (size_t)mb * 64 * 1280 + nb * 160, sink);
            } else {
              wait_tag(wz + WS_FW + ((xc & 1) * 128 + lane_) * 8, wz + WS_FW + ((xc & 1) * 128 + 64 + (lane_ & 3)) * 8, tag);
              const char* wp = wz + WS_WP + (size_t)(xc & 1) * 4 * 68 * 5120 + (size_t)ix * 43520;
              touch_sc1(lt, wp, sink);
              if (wq < 2) touch_sc1(lt < 43520 - 32768 ? lt : 0u, wp + 32768, sink);
            }
          }
         } else {
          FRESH();
          if (c == 0) {
            AT_ISSUE(2, AT_CH); AT_ISSUE(3, 2 * AT_CH);
            if (wq == 0 && lane_ < 16) *(LAS unsigned*)(lds + AT_SY + lane_ * 4) = 0u;
        if (pass == 0 && bid_ < 136) {
          const int ks = bid_ >> 1;
          unsigned v[5];
#pragma unroll
          for (int j = 0; j < 5; ++j) {
            const int u = j * 64 + lane_, pr = u / 10, pos = u - pr * 10, k = pr ^ (((pr >> 3) & 1) << 2);
            const int row = ks < 48 ? k : ks < 64 ? (k >> 4) * 20 + (k & 15) : (k >> 2) * 20 + 16 + (k & 3);
            v[j] = row * 1280 + pos * 16 - 1024 * (j & 3);
          }
          const char* src = ks < 48 ? wd + (size_t)ks * 32 * 1280 : ks < 64 ? wo + (size_t)(ks - 48) * 40 * 1280 : wo + (size_t)(ks - 64) * 160 * 1280;
          glds5(v, rflp(src + ((bid_ & 1) * 4 + wq) * 160), rfl(ldsb + AT_RP + wq * 5120));
        }
          } else if (c == 1) {
            AT_ISSUE(4, 3 * AT_CH); AT_ISSUE(5, AT_A);
            if (pass == 0) {
              if (wq == 0 && lane_ == 0) {
                const int t_ = (xc & 1) * 30 + ix;
                set_flag(wz + WS_FH + (((xc >> 1) * 4 + (t_ & 3)) * 16 + (xc & 1) * 8 + (ix >> 2)) * 8, tag);
              }
              if (bid_ < 136) {
                u32x4 t[5];
#pragma unroll
                for (int j = 0; j < 5; ++j) {
                  const int r = lane_ & 15, g = lane_ >> 4;
                  const lds_ptr w0 = lds + AT_RP + wq * 5120 + (8 * g + ((r >> 2) ^ (4 * (g & 1)))) * 160 + (r & 3) * 8 + j * 32;
                  const lds_ptr w1 = lds + AT_RP + wq * 5120 + (8 * g + ((4 + (r >> 2)) ^ (4 * (g & 1)))) * 160 + (r & 3) * 8 + j * 32;
                  t[j] = __builtin_bit_cast(u32x4, cat8(ldstr(w0), ldstr(w1)));
                }
#pragma unroll
                for (int j = 0; j < 5; ++j) st16_sc1(wz + WS_WP + (size_t)(((bid_ & 1) * 4 + wq) * 68 + (bid_ >> 1)) * 5120, vl + j * 1024, t[j]);
              }
            }
          } else if (c == 2) {
            AT_ISSUE(6, AT_6); AT_ISSUE(7, AT_7);
          } else {
            if (pass == 0 && bid_ < 136 && wq == 0 && lane_ == 0) set_flag(wz + WS_FW + ((bid_ & 1) * 128 + (bid_ >> 1)) * 8, tag);
          }
         }
        }
        SBAR();
        UNIT(0, 1, 3, do { ka = LDKA(kn, 0); kb = lds128(kbn); } while (0), LDV_(3));
        UNIT(1, 0, 0, (void)0, (void)0);
        sl_cur = sl_next;
        sl_next = ((unsigned)tbl & 1023u) << 8;
        tbl >>= 10;
      }
      oacc[1] = MFMA32(vf[0], PFR(1, 0), oacc[1]);
      oacc[1] = MFMA32(vf[1], PFR(1, 1), oacc[1]);
      f32x4 o2; float lpart;
#pragma unroll
      for (int e = 0; e < 4; ++e) {
        auto sw = __builtin_amdgcn_permlane32_swap(f2u(oacc[0][8 + e]), f2u(oacc[1][8 + e]), false, false);
        o2[e] = u2f(sw[0]);
        if (e == 0) lpart = u2f(sw[1]);
      }
      TS();
      {
        FRESH();
        volatile LAS unsigned* sy = (volatile LAS unsigned*)(lds + AT_SY);
        const lds_ptr ex = lds + AT_EX + wq * 5376;
        if (kh) {
#pragma unroll
          for (int i = 0; i < 4; ++i) *(LAS f32x4*)(ex + i * 1024 + vl) = f32x4{oacc[i >> 1][4 * (i & 1)], oacc[i >> 1][4 * (i & 1) + 1], oacc[i >> 1][4 * (i & 1) + 2], oacc[i >> 1][4 * (i & 1) + 3]};
          *(LAS f32x4*)(ex + 4096 + vl) = o2; *(LAS float*)(ex + 5120 + lane_ * 4) = lpart;
          asm volatile("s_waitcnt vmcnt(0) lgkmcnt(0)" ::: "memory");
          if (lane_ == 0) sy[8 + wq] = 1u;
          __builtin_amdgcn_s_setprio(DPRIO);
          if (bid_ == 255 && wq == 0) {
#pragma unroll 1
            for (int i = 0; i < 2; ++i) wait_tag(wz + WS_FH + (i * 128 + lane_) * 8, wz + WS_FH + (i * 128 + 64 + lane_) * 8, tag);
            if (lane_ == 0) set_flag(wz + WS_EP, tag);
          }
          const __amdgpu_buffer_rsrc_t rs = __builtin_amdgcn_make_buffer_rsrc(wz, 0, (int)WS_DBG, 0x00020000);
          unsigned xo[4];
#pragma unroll
          for (int i = 0; i < 4; ++i) xo[i] = (unsigned)WS_H + (mb * 4 + (i ^ wq)) * 49152 + wq * 1024 + vl;
          const unsigned wo_ = (unsigned)WS_WP + (nb * 68 + wq) * 5120 + vl;
          int sn = ((ix >> 3) * 3 + (ix & 7)) % 12;
          f32x4 ac4[5][4];
#pragma unroll
          for (int i = 0; i < 5; ++i)
#pragma unroll
            for (int j = 0; j < 4; ++j) ac4[i][j] = f32x4{0.f, 0.f, 0.f, 0.f};
          u32x4 hx[2][4], hw_[2][5];
#define H_LOAD(B)                                                                                              \
  do {                                                                                                         \
    _Pragma("unroll") for (int i = 0; i < 5; ++i) {                                                            \
      hw_[B][i] = __builtin_amdgcn_raw_buffer_load_b128(rs, wo_ + i * 1024, sn * 20480, 16);                   \
      if (i < 4) hx[B][i] = __builtin_amdgcn_raw_buffer_load_b128(rs, xo[i], sn * 4096, 16);                   \
    }                                                                                                          \
    sn = sn == 11 ? 0 : sn + 1;                                                                                \
  } while (0)
#define H_MMA(B)                                                                                               \
  do {                                                                                                         \
    _Pragma("unroll") for (int nt = 0; nt < 5; ++nt)                                                           \
      _Pragma("unroll") for (int mt = 0; mt < 4; ++mt)                                                         \
        ac4[nt][mt] = MFMA16(__builtin_bit_cast(bf16x8, hw_[B][nt]), __builtin_bit_cast(bf16x8, hx[B][mt]), ac4[nt][mt]); \
  } while (0)
          H_LOAD(0); H_LOAD(1);
#pragma unroll 1
          for (int it = 0; it < 5; ++it) {
            SBAR(); H_MMA(0); SBAR(); H_LOAD(0);
            SBAR(); H_MMA(1); SBAR(); H_LOAD(1);
          }
          SBAR(); H_MMA(0); H_MMA(1);
          f32x4 acc[5], acs[5];
          _Pragma("unroll 1") for (unsigned spins = 0; spins < (kSpinCap << 6); ++spins) {
            if (rfl(read_sy(lds + D_SY + 32))) break;
            __builtin_amdgcn_s_sleep(1);
          }
#pragma unroll
          for (int nt = 0; nt < 5; ++nt) {
            *(LAS f32x4*)(lds + D_PS + (wq ^ 2) * 10240 + nt * 1024 + vl) = ac4[nt][2];
            *(LAS f32x4*)(lds + D_PS + (wq ^ 2) * 10240 + 5120 + nt * 1024 + vl) = ac4[nt][3];
            acc[nt] = ac4[nt][0]; acs[nt] = ac4[nt][1];
          }
          WLB();
          if (!rfl(read_sy(lds + D_SY))) {
    const int r = lane_ & 15, g = lane_ >> 4;
    const unsigned xo0 = (unsigned)WS_O + (mb * 4 + wq) * 20480 + (wq & 1) * 1024 + vl, xo1 = (unsigned)WS_O + (mb * 4 + (wq ^ 1)) * 20480 + (wq & 1) * 1024 + vl;
    const lds_ptr wa = lds + (wq & 1) * 5120 + vl;
    bf16x8 wf[2][5];
    u32x2 xres[5];
#pragma unroll
    for (int nt = 0; nt < 5; ++nt) {
      acc[nt] += *(const LAS f32x4*)(lds + D_PS + wq * 10240 + nt * 1024 + vl);
      acs[nt] += *(const LAS f32x4*)(lds + D_PS + wq * 10240 + 5120 + nt * 1024 + vl);
      xres[nt] = *(const LAS u32x2*)(lds + SCR + 4096 + wq * 3072 + r * 160 + g * 8 + nt * 32);
      acc[nt] += f32x4{bflo(xres[nt][0]), bfhi(xres[nt][0]), bflo(xres[nt][1]), bfhi(xres[nt][1])};
      wf[0][nt] = lds128(wa + nt * 1024);
      PIN(acc[nt]); PIN(acs[nt]); PIN(wf[0][nt]);
    }
    wait_tag(wz + WS_FO4 + mb * 256 + (lane_ & 31) * 8, tag);
    TS();
    u32x4 ox[10][2];
#pragma unroll
    for (int j = 0; j < 10; ++j) {
      ox[j][0] = __builtin_amdgcn_raw_buffer_load_b128(rs, xo0 + j * 2048, 0, 16);
      ox[j][1] = __builtin_amdgcn_raw_buffer_load_b128(rs, xo1 + j * 2048, 0, 16);
    }
    SBAR();
#pragma unroll
    for (int j = 0; j < 10; ++j) {
      if (j < 9) {
#pragma unroll
        for (int nt = 0; nt < 5; ++nt) wf[(j + 1) & 1][nt] = lds128(wa + (j + 1) * 10240 + nt * 1024);
      }
#pragma unroll
      for (int nt = 0; nt < 5; ++nt) {
        acs[nt] = MFMA16(wf[j & 1][nt], __builtin_bit_cast(bf16x8, ox[j][1]), acs[nt]);
        acc[nt] = MFMA16(wf[j & 1][nt], __builtin_bit_cast(bf16x8, ox[j][0]), acc[nt]);
      }
      SBAR();
    }
#pragma unroll
    for (int nt = 0; nt < 5; ++nt) *(LAS f32x4*)(lds + D_PS + wq * 10240 + nt * 1024 + vl) = acs[nt];
    WLB();
#pragma unroll
    for (int nt = 0; nt < 5; ++nt) acc[nt] += *(const LAS f32x4*)(lds + D_PS + (wq ^ 1) * 10240 + nt * 1024 + vl);
    unsigned w[5][2];
#pragma unroll
    for (int nt = 0; nt < 5; ++nt) {
      w[nt][0] = cvtpk(acc[nt][0], acc[nt][1]);
      w[nt][1] = cvtpk(acc[nt][2], acc[nt][3]);
    }
    char* op = out + ((size_t)((mb * 64 + wq * 16 + r) * 640 + nb * 80)) * 2;
#pragma unroll
    for (int j = 0; j < 2; ++j) {
      auto s0 = __builtin_amdgcn_permlane16_swap(w[2 * j][0], w[2 * j + 1][0], false, false);
      auto s1 = __builtin_amdgcn_permlane16_swap(w[2 * j][1], w[2 * j + 1][1], false, false);
      *(u32x4*)(op + j * 64 + (g & 1) * 32 + (g >> 1) * 16) = u32x4{s0[0], s1[0], s0[1], s1[1]};
    }
    *(u32x2*)(op + 128 + g * 8) = u32x2{w[4][0], w[4][1]};
            FINISH();
          }
        } else {
          _Pragma("unroll 1") for (unsigned spins = 0; spins < (kSpinCap << 6); ++spins) {
            if (rfl(sy[8 + wq])) break;
            __builtin_amdgcn_s_sleep(1);
          }
          asm volatile("" ::: "memory");
          f32x4 p[5];
#pragma unroll
          for (int i = 0; i < 5; ++i) p[i] = *(const LAS f32x4*)(ex + i * 1024 + vl);
          const float lsum = adds(lpart, *(const LAS float*)(ex + 5120 + lane_ * 4));
          const bool bad = pass == 0 && wany(!(lsum >= 0x1p-70f && lsum <= 0x1p40f));
          const int head = xc * 4 + (ix >> 3), qb = ix & 7, r32 = lane_ & 31, hh = lane_ >> 5;
          const float rl = __builtin_amdgcn_rcpf(lsum);
          auto sr = __builtin_amdgcn_permlane32_swap(f2u(rl), f2u(rl), false, false);
#pragma unroll
          for (int a = 0; a < 2; ++a) {
            const float ra = u2f(sr[a]);
            unsigned w[4];
#pragma unroll
            for (int i = 0; i < 4; ++i)
              w[i] = cvtpk(muls(adds(oacc[a][2 * i], p[2 * a + (i >> 1)][2 * (i & 1)]), ra), muls(adds(oacc[a][2 * i + 1], p[2 * a + (i >> 1)][2 * (i & 1) + 1]), ra));
            auto s0 = __builtin_amdgcn_permlane32_swap(w[0], w[2], false, false);
            auto s1 = __builtin_amdgcn_permlane32_swap(w[1], w[3], false, false);
            const int tok = qb * 256 + wq * 64 + a * 32 + r32;
            st16_sc1(wz + WS_O, (((tok >> 4) * 80 + head * 2 + hh) * 16 + (tok & 15)) * 16, u32x4{s0[0], s1[0], s0[1], s1[1]});
          }
          const int tok = qb * 256 + wq * 64 + lane_;
          st8_sc1(wz + WS_O + ((size_t)((tok >> 4) * 80 + 64 + (head >> 1)) * 16 + (tok & 15)) * 16 + (head & 1) * 8,
                  u32x2{cvtpk(muls(adds(o2[0], p[4][0]), rl), muls(adds(o2[1], p[4][1]), rl)), cvtpk(muls(adds(o2[2], p[4][2]), rl), muls(adds(o2[3], p[4][3]), rl))});
          unsigned cnt = 0;
          if (lane_ == 0) cnt = __hip_atomic_fetch_add((LAS unsigned*)(lds + AT_SY + 48), bad ? 0x101u : 1u, __ATOMIC_RELAXED, __HIP_MEMORY_SCOPE_WORKGROUP) + (bad ? 0x101u : 1u);
          cnt = rfl(cnt);
          if ((cnt & 255u) == 4u && lane_ == 0) {
            *(volatile LAS unsigned*)(lds + D_SY) = cnt >> 8;
            *(volatile LAS unsigned*)(lds + D_SY + 32) = 1u;
          }
          asm volatile("s_waitcnt vmcnt(0)" ::: "memory");
          if (!bad && lane_ == 0) set_flag(wz + WS_FO4 + ((qb * 4 + wq) * 32 + head) * 8, tag);
          _Pragma("unroll 1") for (unsigned spins = 0; spins < (kSpinCap << 6); ++spins) {
            if (rfl(read_sy(lds + D_SY + 32))) break;
            __builtin_amdgcn_s_sleep(1);
          }
          {
            const char* wp = wz + WS_WP + (size_t)(nb * 68 + 48) * 5120 + wq * 25600;
#pragma unroll
            for (int i = 0; i < 6; ++i) glds4_sc1(vl, vl, vl, vl, wp + i * 4096, rfl(ldsb + wq * 25600 + i * 4096));
            glds1_sc1(vl, wp + 24576, rfl(ldsb + wq * 25600 + 24576));
          }
          {
            unsigned v[3];
#pragma unroll
            for (int j = 0; j < 3; ++j) { const int u = min(j * 64 + lane_, 159), row = u / 10; v[j] = row * 1280 + (u - row * 10) * 16; }
            const char* xp = x + (size_t)(mb * 64 + wq * 16) * 1280 + nb * 160;
            glds2(v[0], v[1] - 1024, xp, rfl(ldsb + SCR + 4096 + wq * 3072));
            glds1_sc1(v[2], xp, rfl(ldsb + SCR + 4096 + wq * 3072 + 2048));
          }
          WVB(0);
          if (!rfl(read_sy(lds + D_SY))) FINISH();
        }
      }
      FRESH();
      __builtin_amdgcn_s_setprio(0);
      WVB(0);
      if (wid < 4) { AT_ISSUE(0, AT_A); AT_ISSUE(1, AT_B); }
      float mx[2] = {-INFINITY, -INFINITY};
      int lane = threadIdx.x & 63;
      PIN(lane);
      const int r32 = lane & 31, hh = lane >> 5;
      for (int gi = 0; gi < 16; ++gi) {
        const char* gp = kvg + (size_t)gi * KVGS;
        kb = __builtin_bit_cast(bf16x8, ld16_sc1_wait(gp + 4096 + lane * 16));
#pragma unroll
        for (int tp = 0; tp < 2; ++tp) {
          const u32x4 k0 = ld16_sc1_wait(gp + hh * 2048 + r32 * 16 + tp * 512), k1 = ld16_sc1_wait(gp + hh * 2048 + r32 * 16 + 1024 + tp * 512);
#pragma unroll
          for (int u = 0; u < 2; ++u) {
            ka = __builtin_bit_cast(bf16x8, u32x4{k0[2 * u], k0[2 * u + 1], k1[2 * u], k1[2 * u + 1]});
#pragma unroll
            for (int a = 0; a < 2; ++a) {
              f32x16 s = MFMA32(ka, qa[a], f32x16{});
              s = MFMA32K8(KBH(u), qv[a][tp], s);
#pragma unroll
              for (int e = 0; e < 16; ++e) mx[a] = fmaxf(mx[a], s[e]);
            }
          }
        }
      }
#pragma unroll
      for (int a = 0; a < 2; ++a) mrow[a] = maxx32(mx[a]);
      WVB(0);
    }
  }
#undef AT_ISSUE
#undef UNIT

}

extern "C" void kernel_launch(void* const* d_in, const int* in_sizes, int n_in, void* d_out, int out_size, void* d_ws, size_t ws_size,
                              hipStream_t stream) {
  fused_block<<<256, 512, 0, stream>>>((const char*)d_in[0], (const char*)d_in[1], (const char*)d_in[2], (const char*)d_in[4],
                                       (const char*)d_in[5], (const char*)d_in[6], (char*)d_ws, (char*)d_out);
}
```

```cpp
#include <hip/hip_runtime.h>
#include <hip/hip_bf16.h>
#include <stdint.h>

#define DI __device__ __forceinline__
#define LAS __attribute__((address_space(3)))
#define GAS __attribute__((address_space(1)))
typedef short bf16x8 __attribute__((ext_vector_type(8)));
typedef short s16x4 __attribute__((ext_vector_type(4)));
typedef float f32x4 __attribute__((ext_vector_type(4)));
typedef float f32x2 __attribute__((ext_vector_type(2)));
typedef float f32x16 __attribute__((ext_vector_type(16)));
typedef unsigned u32x4 __attribute__((ext_vector_type(4)));
typedef unsigned u32x2 __attribute__((ext_vector_type(2)));
typedef __bf16 bf16x2_t __attribute__((ext_vector_type(2)));
typedef LAS char* lds_ptr;
typedef unsigned long long u64;
typedef GAS u64 gu64;

constexpr int S = 2048, D = 640, F = 1536;
constexpr size_t QGS = 5120, QHS = 16 * QGS;
constexpr size_t KVGS = 10752, KVHS = 16 * KVGS;
constexpr size_t WS_EP = 0;
constexpr size_t WS_FQ = 4096;
constexpr size_t WS_FH = WS_FQ + 2048;
constexpr size_t WS_FW = WS_FH + 4096;
constexpr size_t WS_FO = 1032192;
constexpr size_t WS_ST = 16384;
constexpr size_t WS_CG = WS_ST + 2048 * 8;
constexpr size_t WS_KP = WS_CG + 16 * 3072 * 8;
constexpr size_t WS_CS = WS_KP + 32 * 32 * 8 * 4;
constexpr size_t WS_Q = 1048576;
constexpr size_t WS_KV = WS_Q + 32 * QHS;
constexpr size_t WS_H = WS_KV + 32 * KVHS + 4096;
constexpr size_t WS_O = WS_H + (size_t)S * F * 2;
constexpr size_t WS_WP = WS_O + (size_t)S * D * 2;
constexpr size_t WS_END = WS_WP + 8 * 68 * 5120;
static_assert(WS_CS + 15 * 16 * 128 * 8 <= WS_FO && WS_FO + 8192 <= WS_Q, "ws map");

#define SBAR() __builtin_amdgcn_sched_barrier(0)
#define PIN(x) asm volatile("" : "+v"(x))
#define WVB(N) wait_vm_bar_<N>()
#define WLB() wait_lgkm_bar_()
#define BAR() bar_()
#define MFMA16(a, b, c) __builtin_amdgcn_mfma_f32_16x16x32_bf16(a, b, c, 0, 0, 0)
#define MFMA32(a, b, c) __builtin_amdgcn_mfma_f32_32x32x16_bf16(a, b, c, 0, 0, 0)
#define MFMA32K8(a, b, c) __builtin_amdgcn_mfma_f32_32x32x8bf16_1k(a, b, c, 0, 0, 0)
#define RLX_AGENT __ATOMIC_RELAXED, __HIP_MEMORY_SCOPE_AGENT
#define CODE_LINES 282
constexpr unsigned kSpinCap = 1u << 17;
constexpr int LDS_BYTES = 163840;

DI unsigned f2u(float x) { return __builtin_bit_cast(unsigned, x); }
DI float u2f(unsigned x) { return __builtin_bit_cast(float, x); }
DI bool wall(bool p) { return __builtin_amdgcn_ballot_w64(!p) == 0; }
DI bool wany(bool p) { return __builtin_amdgcn_ballot_w64(p) != 0; }
DI float adds(float a, float b) { float r; asm("v_add_f32 %0, %1, %2" : "=v"(r) : "v"(a), "v"(b)); return r; }
DI float muls(float a, float b) { float r; asm("v_mul_f32 %0, %1, %2" : "=v"(r) : "v"(a), "v"(b)); return r; }
DI float fmas(float a, float b, float c) { float r; asm("v_fma_f32 %0, %1, %2, %3" : "=v"(r) : "v"(a), "v"(b), "v"(c)); return r; }
DI unsigned rfl(unsigned x) { return __builtin_amdgcn_readfirstlane(x); }
DI const char* rflp(const char* p) { const u64 u = (u64)p; return (const char*)(((u64)rfl((unsigned)(u >> 32)) << 32) | rfl((unsigned)u)); }
DI unsigned cvtpk(float lo, float hi) { f32x2 v = {lo, hi}; bf16x2_t b = __builtin_convertvector(v, bf16x2_t); return __builtin_bit_cast(unsigned, b); }
DI float bfr(float x) { return u2f(cvtpk(x, 0.f) << 16); }
DI float bflo(unsigned u) { return u2f(u << 16); }
DI float bfhi(unsigned u) { return u2f(u & 0xffff0000u); }
DI bf16x8 lds128(lds_ptr p) { return *(const LAS bf16x8*)p; }
DI s16x4 lds64(lds_ptr p) { return *(const LAS s16x4*)p; }
DI s16x4 ldstr(lds_ptr p) { return __builtin_amdgcn_ds_read_tr16_b64_v4i16((LAS s16x4*)p); }
DI bf16x8 cat8(s16x4 a, s16x4 b) { return __builtin_shufflevector(a, b, 0, 1, 2, 3, 4, 5, 6, 7); }
template <int N> DI void wait_vm_bar_() { asm volatile("s_waitcnt vmcnt(%0) lgkmcnt(0)\n\ts_barrier" ::"i"(N) : "memory"); }
DI void wait_lgkm_bar_() { asm volatile("s_waitcnt lgkmcnt(0)\n\ts_barrier" ::: "memory"); }
DI void st16_sc1(const char* sb, unsigned off, u32x4 v) { asm volatile("global_store_dwordx4 %0, %1, %2 sc1\n\ts_nop 1" ::"v"(off), "v"(v), "s"(sb) : "memory"); }
DI void st8_sc1(char* p, u32x2 v) { asm volatile("global_store_dwordx2 %0, %1, off sc1\n\ts_nop 1" ::"v"(p), "v"(v) : "memory"); }
DI u32x4 ld16_sc1_wait(const char* p) { u32x4 v; asm volatile("global_load_dwordx4 %0, %1, off sc1\n\ts_waitcnt vmcnt(0)" : "=&v"(v) : "v"(p) : "memory"); return v; }

DI void glds2(unsigned v0, unsigned v1, const void* sbase, unsigned m0v) {
  unsigned sv;
  asm volatile("s_mov_b32 %0, m0\n\ts_mov_b32 m0, %4\n\ts_nop 0\n\t"
               "global_load_lds_dwordx4 %1, %3\n\t"
               "global_load_lds_dwordx4 %2, %3 offset:1024\n\t"
               "s_mov_b32 m0, %0"
               : "=&s"(sv) : "v"(v0), "v"(v1), "s"(sbase), "s"(m0v) : "memory");
}
DI void glds5(const unsigned (&v)[5], const void* sbase, unsigned m0v) {
  unsigned sv;
  asm volatile("s_nop 4\n\ts_mov_b32 %0, m0\n\ts_mov_b32 m0, %7\n\ts_nop 0\n\t"
               "global_load_lds_dwordx4 %1, %6\n\t"
               "global_load_lds_dwordx4 %2, %6 offset:1024\n\t"
               "global_load_lds_dwordx4 %3, %6 offset:2048\n\t"
               "global_load_lds_dwordx4 %4, %6 offset:3072\n\t"
               "s_add_u32 m0, m0, 4096\n\ts_nop 0\n\t"
               "global_load_lds_dwordx4 %5, %6\n\t"
               "s_mov_b32 m0, %0"
               : "=&s"(sv) : "v"(v[0]), "v"(v[1]), "v"(v[2]), "v"(v[3]), "v"(v[4]), "s"(sbase), "s"(m0v) : "memory", "scc");
}
DI void wait_tag(const char* p0, const char* p1, u64 tag) {
  _Pragma("unroll 1") for (unsigned spins = 0; spins < kSpinCap; ++spins) {
    const u64 a = __hip_atomic_load((gu64*)p0, RLX_AGENT), b = __hip_atomic_load((gu64*)p1, RLX_AGENT);
    if (wall(a == tag && b == tag)) break;
    __builtin_amdgcn_s_sleep(2);
  }
  __builtin_amdgcn_fence(__ATOMIC_ACQUIRE, "wavefront");
}
template <int SL = 2> DI void wait_tag(const char* p0, u64 tag) {
  _Pragma("unroll 1") for (unsigned spins = 0; spins < kSpinCap; ++spins) {
    const u64 a = __hip_atomic_load((gu64*)p0, RLX_AGENT);
    if (wall(a == tag)) break;
    __builtin_amdgcn_s_sleep(SL);
  }
  __builtin_amdgcn_fence(__ATOMIC_ACQUIRE, "wavefront");
}
DI void set_flag(char* p, u64 tag) { __hip_atomic_store((gu64*)p, tag, RLX_AGENT); }
DI u64 make_tag(u64 e) {
  const u64 u = ((u64)rfl((unsigned)(e >> 32)) << 32) | rfl((unsigned)e);
  return u * 0x9E3779B97F4A7C15ull + 0xD1B54A32D192ED03ull;
}

DI void glds4(unsigned v0, unsigned v1, unsigned v2, unsigned v3, const void* sbase, unsigned m0v) {
  unsigned sv;
  asm volatile("s_mov_b32 %0, m0\n\ts_mov_b32 m0, %6\n\ts_nop 0\n\t"
               "global_load_lds_dwordx4 %1, %5\n\t"
               "global_load_lds_dwordx4 %2, %5 offset:1024\n\t"
               "global_load_lds_dwordx4 %3, %5 offset:2048\n\t"
               "global_load_lds_dwordx4 %4, %5 offset:3072\n\t"
               "s_mov_b32 m0, %0"
               : "=&s"(sv) : "v"(v0), "v"(v1), "v"(v2), "v"(v3), "s"(sbase), "s"(m0v) : "memory");
}
DI void glds4_sc1(unsigned v0, unsigned v1, unsigned v2, unsigned v3, const void* sbase, unsigned m0v) {
  unsigned sv;
  asm volatile("s_mov_b32 %0, m0\n\ts_mov_b32 m0, %6\n\ts_nop 0\n\t"
               "global_load_lds_dwordx4 %1, %5 sc1\n\t"
               "global_load_lds_dwordx4 %2, %5 offset:1024 sc1\n\t"
               "global_load_lds_dwordx4 %3, %5 offset:2048 sc1\n\t"
               "global_load_lds_dwordx4 %4, %5 offset:3072 sc1\n\t"
               "s_mov_b32 m0, %0"
               : "=&s"(sv) : "v"(v0), "v"(v1), "v"(v2), "v"(v3), "s"(sbase), "s"(m0v) : "memory");
}
DI void glds1_sc1(unsigned v0, const void* sbase, unsigned m0v) {
  unsigned sv;
  asm volatile("s_mov_b32 %0, m0\n\ts_mov_b32 m0, %3\n\ts_nop 0\n\tglobal_load_lds_dwordx4 %1, %2 sc1\n\ts_mov_b32 m0, %0"
               : "=&s"(sv) : "v"(v0), "s"(sbase), "s"(m0v) : "memory");
}
DI void touch(unsigned voff, const void* sbase, unsigned m0v) {
  unsigned sv;
  asm volatile("s_mov_b32 %0, m0\n\ts_mov_b32 m0, %3\n\ts_nop 0\n\tglobal_load_lds_dword %1, %2\n\ts_mov_b32 m0, %0"
               : "=&s"(sv) : "v"(voff), "s"(sbase), "s"(m0v) : "memory");
}
DI void touch_sc1(unsigned voff, const void* sbase, unsigned m0v) {
  unsigned sv;
  asm volatile("s_mov_b32 %0, m0\n\ts_mov_b32 m0, %3\n\ts_nop 0\n\tglobal_load_lds_dword %1, %2 sc1\n\ts_mov_b32 m0, %0"
               : "=&s"(sv) : "v"(voff), "s"(sbase), "s"(m0v) : "memory");
}
DI void st_gran(char* p, unsigned val, u64 tag) { __hip_atomic_store((gu64*)p, ((u64)(unsigned)tag << 32) | val, RLX_AGENT); }
#define SGB(mask, n) __builtin_amdgcn_sched_group_barrier(mask, n, 0)

constexpr int WR = 65536;
constexpr int SCR = 147456;
constexpr int GWS = WR + 65536, GWA = WR + 16384;
constexpr int SCR_Q = WR;
constexpr int SCR_STAT = 159744, SCR_CS = SCR_STAT + 1024, SCR_CG = SCR_STAT + 1536, SCR_KN = SCR_STAT + 2320, SCR_M = SCR_STAT + 2560;
constexpr int SCR_DUMMY = 163584;
constexpr int AT_CH = 2 * (int)KVGS;
constexpr int AT_A = 0, AT_B = 86016;
constexpr int AT_6 = 142336, AT_7 = 120576;
constexpr int AT_RP = AT_7;
constexpr int AT_EX = AT_B;
constexpr int AT_SY = 107520;
constexpr int D_PS = 102400;
constexpr int D_SY = AT_6 + 5120 + 1344;


template <int CTRL> DI float dpp(float x) { return u2f(__builtin_amdgcn_update_dpp(0u, f2u(x), CTRL, 0xf, 0xf, true)); }
DI float sum8(float x) { x += dpp<0xB1>(x); x += dpp<0x4E>(x); x += dpp<0x141>(x); return x; }
DI float sum16(float x) { x = sum8(x); x += dpp<0x140>(x); return x; }
DI float max16(float x) { x = fmaxf(x, dpp<0xB1>(x)); x = fmaxf(x, dpp<0x4E>(x)); x = fmaxf(x, dpp<0x141>(x)); x = fmaxf(x, dpp<0x140>(x)); return x; }
template <int CTRL> DI unsigned dppu(unsigned x) { return __builtin_amdgcn_update_dpp(0u, x, CTRL, 0xf, 0xf, true); }
DI unsigned umax16(unsigned x) { x = max(x, dppu<0xB1>(x)); x = max(x, dppu<0x4E>(x)); x = max(x, dppu<0x141>(x)); x = max(x, dppu<0x140>(x)); return x; }
DI float sumx16(float x) { auto r = __builtin_amdgcn_permlane16_swap(f2u(x), f2u(x), false, false); return u2f(r[0]) + u2f(r[1]); }
DI float sumx32(float x) { auto r = __builtin_amdgcn_permlane32_swap(f2u(x), f2u(x), false, false); return adds(u2f(r[0]), u2f(r[1])); }
DI float maxx16(float x) { auto r = __builtin_amdgcn_permlane16_swap(f2u(x), f2u(x), false, false); return fmaxf(u2f(r[0]), u2f(r[1])); }
DI float maxx32(float x) { auto r = __builtin_amdgcn_permlane32_swap(f2u(x), f2u(x), false, false); return fmaxf(u2f(r[0]), u2f(r[1])); }
DI u32x4 bld16_sc1(__amdgpu_buffer_rsrc_t rs, unsigned off) { return __builtin_amdgcn_raw_buffer_load_b128(rs, off, 0, 16); }
DI u32x2 bld8_sc1(__amdgpu_buffer_rsrc_t rs, unsigned off) { return __builtin_amdgcn_raw_buffer_load_b64(rs, off, 0, 16); }
DI float sq2(unsigned u, float c) { const bf16x2_t v = __builtin_bit_cast(bf16x2_t, u); return __builtin_amdgcn_fdot2_f32_bf16(v, v, c, false); }
DI unsigned read_sy(lds_ptr p) { return *(volatile LAS unsigned*)p; }
DI void bar_() { asm volatile("s_barrier" ::: "memory"); }

struct Pend { u32x4 v[4][2]; unsigned off[4]; const char* base; unsigned str; };
template <bool SWAP>
DI void qkv_compute(lds_ptr lds, int cw, int lane, int rb, int cb, char* ws, Pend& pd) {
  const int wm = cw >> 1, wn = cw & 1, r = lane & 15, g = lane >> 4;
  lds_ptr xa[2], wa[4];
#pragma unroll
  for (int ks = 0; ks < 2; ++ks) xa[ks] = lds + (wm * 64 + r) * 128 + (((4 * ks + g) ^ ((r >> 1) & 7)) * 16);
  {
    const int q = r >> 2, p = r & 3, f = q | ((g & 1) << 2);
#pragma unroll
    for (int nt = 0; nt < 4; ++nt) wa[nt] = lds + WR + (8 * g + q) * 256 + (((wn * 4 + nt) ^ f) * 32) + p * 8;
  }
  f32x4 acc[4][4];
#pragma unroll
  for (int i = 0; i < 4; ++i)
#pragma unroll
    for (int j = 0; j < 4; ++j) acc[i][j] = f32x4{0.f, 0.f, 0.f, 0.f};
  bf16x8 xf[2][4], wf[2][4];
#define Q_READ(B, SO, KS)                                                                                           \
  do {                                                                                                              \
    _Pragma("unroll") for (int i = 0; i < 4; ++i) {                                                                 \
      wf[B][i] = cat8(ldstr(wa[i] + (((SO) + 49152) & 65535) + (KS) * 8192), ldstr(wa[i] + (((SO) + 49152) & 65535) + (KS) * 8192 + 1024)); \
      xf[B][i] = lds128(xa[KS] + (SO) + i * 2048);                                                                  \
    }                                                                                                               \
  } while (0)
#define Q_MMA(B)                                                                                                    \
  do {                                                                                                              \
    _Pragma("unroll") for (int nt = 0; nt < 4; ++nt)                                                                \
      _Pragma("unroll") for (int mt = 0; mt < 4; ++mt)                                                              \
        acc[nt][mt] = SWAP ? MFMA16(wf[B][nt], xf[B][mt], acc[nt][mt]) : MFMA16(xf[B][mt], wf[B][nt], acc[nt][mt]); \
  } while (0)
#define Q_MIX() do { _Pragma("unroll") for (int i = 0; i < 12; ++i) { SGB(0x8, 1); SGB(0x100, 1); } SGB(0x8, 4); } while (0)
  WVB(1);
  Q_READ(0, 0, 0);
  int so = 0;
  SBAR();
  Q_READ(1, 0, 1); Q_MMA(0); Q_MIX();
#pragma unroll 1
  for (int st = 0; st < 9; ++st) {
    SBAR();
    WLB();
    so = (so + 16384) & 65535;
    SBAR();
    Q_READ(0, so, 0); Q_MMA(1); Q_MIX();
    SBAR();
    Q_READ(1, so, 1); Q_MMA(0); Q_MIX();
  }
  SBAR();
  Q_MMA(1);
#undef Q_READ
#undef Q_MMA
#undef Q_MIX
  const LAS f32x2* stat = (const LAS f32x2*)(lds + SCR_STAT);
  const LAS float* csum = (const LAS float*)(lds + SCR_CS);
  const int kind = cb / 5, fb = (cb - kind * 5) * 128 + wn * 64;
  if (SWAP) {
    f32x2 st4[4];
    const float qs = kind == 0 ? 0.22360679774997896f * 1.4426950408889634f : 1.0f;
#pragma unroll
    for (int mt = 0; mt < 4; ++mt) { st4[mt] = stat[wm * 64 + mt * 16 + r]; st4[mt][1] *= qs; }
    const char* img = kind == 0 ? ws + WS_Q + (size_t)rb * QGS : ws + WS_KV + (size_t)rb * KVGS;
    const unsigned hs = kind == 0 ? (unsigned)QHS : (unsigned)KVHS;
    const unsigned rowoff = kind == 0 ? r * 16 : ((r >> 3) * 16 + (r & 7)) * 16, mstr = kind == 0 ? 256 : 128;
    float nmax[4]; unsigned kpo[4];
#pragma unroll
    for (int nt = 0; nt < 4; ++nt) {
      const int nl = nt * 16 + 4 * g;
      const f32x4 cs = *(const LAS f32x4*)(csum + wn * 64 + nl);
      const int f0 = fb + nl, head = f0 / 20, sl = (f0 - head * 20) >> 2;
      const unsigned base = head * hs + sl * 1024 + wm * 512 + rowoff;
      nmax[nt] = 0.f; kpo[nt] = (head * 32 + rb * 2 + wm) * 8 + sl;
      unsigned pk[4][2];
#pragma unroll
      for (int mt = 0; mt < 4; ++mt) {
        float v[4];
#pragma unroll
        for (int e = 0; e < 4; ++e) v[e] = (acc[nt][mt][e] - st4[mt][0] * cs[e]) * st4[mt][1];
        pk[mt][0] = cvtpk(v[0], v[1]); pk[mt][1] = cvtpk(v[2], v[3]);
        nmax[nt] = fmaxf(nmax[nt], sq2(pk[mt][1], sq2(pk[mt][0], 0.f)));
      }
      pd.off[nt] = base;
      pd.v[nt][0] = u32x4{pk[0][0], pk[0][1], pk[2][0], pk[2][1]};
      pd.v[nt][1] = u32x4{pk[1][0], pk[1][1], pk[3][0], pk[3][1]};
    }
    pd.base = img; pd.str = mstr;
    if (kind == 1) {
      const bool h0 = r & 1, h1 = r & 2;
#pragma unroll
      for (int nt = 0; nt < 4; ++nt) nmax[nt] = u2f(umax16(f2u(nmax[nt])));
      const float a = h1 ? (h0 ? nmax[3] : nmax[2]) : (h0 ? nmax[1] : nmax[0]);
      const unsigned o = h1 ? (h0 ? kpo[3] : kpo[2]) : (h0 ? kpo[1] : kpo[0]);
      if (r < 4) __hip_atomic_store((GAS float*)(ws + WS_KP) + o, a, RLX_AGENT);
    }
  } else {
    const char* img = ws + WS_KV + (size_t)rb * KVGS + 5120;
    unsigned off[4]; float cs[4];
#pragma unroll
    for (int nt = 0; nt < 4; ++nt) {
      const int nl = nt * 16 + r, f = fb + nl, head = f / 20, d = f - head * 20;
      cs[nt] = csum[wn * 64 + nl];
      off[nt] = head * (unsigned)KVHS + wm * 2816 + d * 64 + ((g ^ ((d >> 2) & 3)) * 16);
    }
#pragma unroll
    for (int j = 0; j < 2; ++j) {
      f32x4 sa[2], sb[2];
#pragma unroll
      for (int i = 0; i < 2; ++i) {
        sa[i] = *(const LAS f32x4*)(stat + wm * 64 + (2 * j + i) * 16 + 4 * g);
        sb[i] = *(const LAS f32x4*)(stat + wm * 64 + (2 * j + i) * 16 + 4 * g + 2);
      }
#pragma unroll
      for (int nt = 0; nt < 4; ++nt) {
        u32x4 o;
#pragma unroll
        for (int i = 0; i < 2; ++i) {
          const f32x4 a = acc[nt][2 * j + i];
          o[2 * i] = cvtpk((a[0] - sa[i][0] * cs[nt]) * sa[i][1], (a[1] - sa[i][2] * cs[nt]) * sa[i][3]);
          o[2 * i + 1] = cvtpk((a[2] - sb[i][0] * cs[nt]) * sb[i][1], (a[3] - sb[i][2] * cs[nt]) * sb[i][3]);
        }
        pd.v[nt][j] = o;
      }
    }
#pragma unroll
    for (int nt = 0; nt < 4; ++nt) pd.off[nt] = off[nt];
    pd.base = img; pd.str = 1408;
    {
      const int t = cw * 64 + lane, h0 = ((cb - 10) * 128) / 20, h1 = ((cb - 9) * 128) / 20, hd = h0 + (t >> 5);
      const unsigned c = (t & 4) ? 0u : 0x3f803f80u;
      if (hd < h1) st16_sc1(img, hd * (unsigned)KVHS + ((t >> 3) & 3) * 1408 + 1280 + (t & 7) * 16, u32x4{c, c, c, c});
    }
  }
}

DI void gu_compute(lds_ptr lds, int cw, int lane, int rb, int fbk, char* ws, const Pend& pd, bool has_pd) {
  const int wm = cw >> 1, wn = cw & 1, r = lane & 15, g = lane >> 4;
  lds_ptr xa[2], wa[3];
#pragma unroll
  for (int ks = 0; ks < 2; ++ks) xa[ks] = lds + (wm * 64 + r) * 128 + (((4 * ks + g) ^ ((r >> 1) & 7)) * 16);
#pragma unroll
  for (int nt = 0; nt < 3; ++nt) wa[nt] = lds + (8 * g + (r >> 2)) * 192 + (((wn * 3 + nt) ^ (g & 1)) * 32) + (r & 3) * 8;
  f32x4 acc[6][4];
#pragma unroll
  for (int i = 0; i < 6; ++i)
#pragma unroll
    for (int j = 0; j < 4; ++j) acc[i][j] = f32x4{0.f, 0.f, 0.f, 0.f};
  bf16x8 xf[2][4], wf[2][6];
#define G_READ(B, SO, WO, KS)                                                                                           \
  do {                                                                                                              \
    _Pragma("unroll") for (int i = 0; i < 6; ++i) {                                                                 \
      wf[B][i] = cat8(ldstr(wa[i % 3] + (WO) + (i / 3) * 12288 + (KS) * 6144), ldstr(wa[i % 3] + (WO) + (i / 3) * 12288 + (KS) * 6144 + 768)); \
      if (i < 4) xf[B][i] = lds128(xa[KS] + (SO) + i * 2048);                                                       \
    }                                                                                                               \
  } while (0)
#define G_MMA(B)                                                                                                    \
  do {                                                                                                              \
    _Pragma("unroll") for (int nt = 0; nt < 6; ++nt)                                                                \
      _Pragma("unroll") for (int mt = 0; mt < 4; ++mt) acc[nt][mt] = MFMA16(wf[B][nt], xf[B][mt], acc[nt][mt]);     \
  } while (0)
#define G_MIX() do { _Pragma("unroll") for (int i = 0; i < 16; ++i) { SGB(0x8, 1); SGB(0x100, 1); } SGB(0x8, 8); } while (0)
  int so = 32768, wo = GWS;
  if (has_pd) {
#pragma unroll
    for (int i = 0; i < 4; ++i) { st16_sc1(pd.base, pd.off[i], pd.v[i][0]); st16_sc1(pd.base + pd.str, pd.off[i], pd.v[i][1]); }
  }
  WLB();
  G_READ(0, so, wo, 0);
  SBAR();
  G_READ(1, so, wo, 1); G_MMA(0); G_MIX();
#pragma unroll 1
  for (int st = 10; st < 19; ++st) {
    SBAR();
    if (st == 11) asm volatile("s_waitcnt vmcnt(0)" ::: "memory");
    WLB();
    so = (so + 16384) & 65535;
    wo = wo == GWS ? GWA : wo + 24576;
    SBAR();
    G_READ(0, so, wo, 0); G_MMA(1); G_MIX();
    SBAR();
    G_READ(1, so, wo, 1); G_MMA(0); G_MIX();
  }
  SBAR();
  WLB();
  G_MMA(1);
#undef G_READ
#undef G_MMA
#undef G_MIX
  const LAS f32x2* stat = (const LAS f32x2*)(lds + SCR_STAT);
  const LAS float* csum = (const LAS float*)(lds + SCR_CG);
  f32x2 st4[4];
#pragma unroll
  for (int mt = 0; mt < 4; ++mt) { st4[mt] = stat[wm * 64 + mt * 16 + r]; st4[mt][1] *= st4[mt][1]; }
  const unsigned ho = (((rb * 8 + wm * 4 + (g & 1)) * 192 + fbk * 12 + wn * 6 + (g >> 1)) * 16 + r) * 16;
#pragma unroll
  for (int nt = 0; nt < 3; ++nt) {
    const f32x4 cg = *(const LAS f32x4*)(csum + wn * 48 + nt * 16 + 4 * g), cu = *(const LAS f32x4*)(csum + 96 + wn * 48 + nt * 16 + 4 * g);
    unsigned w[4][2];
#pragma unroll
    for (int mt = 0; mt < 4; ++mt) {
      float hv[4];
#pragma unroll
      for (int e = 0; e < 4; ++e) {
        const float gv = acc[nt][mt][e] - st4[mt][0] * cg[e], uv = acc[3 + nt][mt][e] - st4[mt][0] * cu[e];
        hv[e] = fmaxf(gv, 0.f) * (uv * st4[mt][1]);
      }
      w[mt][0] = cvtpk(hv[0], hv[1]); w[mt][1] = cvtpk(hv[2], hv[3]);
    }
#pragma unroll
    for (int j = 0; j < 2; ++j) {
      auto s0 = __builtin_amdgcn_permlane16_swap(w[2 * j][0], w[2 * j + 1][0], false, false);
      auto s1 = __builtin_amdgcn_permlane16_swap(w[2 * j][1], w[2 * j + 1][1], false, false);
      st16_sc1(ws + WS_H, ho + j * (2 * 192 * 256) + nt * 512, u32x4{s0[0], s1[0], s0[1], s1[1]});
    }
  }
}

__global__ __launch_bounds__(512, 2) void fused_block(const char* __restrict__ x, const char* __restrict__ wqkv, const char* __restrict__ wo,
                                                      const char* __restrict__ wg, const char* __restrict__ wu, const char* __restrict__ wd,
                                                      char* __restrict__ ws, char* __restrict__ out) {
  __shared__ __attribute__((aligned(16))) char smem[LDS_BYTES];
  lds_ptr lds = (lds_ptr)smem;
  const unsigned ldsb = (unsigned)(size_t)lds;
  const int lane = threadIdx.x & 63, wid = rfl(threadIdx.x >> 6), bid = blockIdx.x;
  const int xcd = bid & 7, idx = bid >> 3;
  const int tq = (xcd & 1) * 30 + idx, cb = tq >> 2, rb = (xcd >> 1) * 4 + (tq & 3);
  const int fbk = (xcd & 1) * 8 + (idx >> 2);
  const int head = xcd * 4 + (idx >> 3), qb = idx & 7;
  const bool has_qkv = idx < 30;
  const int rot_g = ((tq & 3) * 5 + 1) / 2 + (xcd >> 1);
  const int sg0 = rot_g >= 10 ? rot_g - 10 : rot_g, sq0 = (rot_g + cb) % 10;
#define NEXT10(k) ((k) == 9 ? 0 : (k) + 1)
  const int r32 = lane & 31, hh = lane >> 5;
  const char* kvg = ws + WS_KV + (size_t)head * KVHS;
  const unsigned vl = lane * 16;
  u64 tag = 0;
  bf16x8 qa[2]; s16x4 qv[2][2]; float mrow[2];
#define AT_ISSUE(C, SLOT)                                                                                      \
  do {                                                                                                         \
    const char* src_ = kvg + (size_t)(C) * AT_CH;                                                              \
    const unsigned dst_ = ldsb + (SLOT);                                                                       \
    unsigned sv_;                                                                                              \
    asm volatile("s_mov_b32 %0, m0\n\ts_mov_b32 m0, %5\n\ts_nop 0\n\t"                                         \
                 "global_load_lds_dwordx4 %1, %2 sc1\n\tglobal_load_lds_dwordx4 %1, %2 offset:1024 sc1\n\t"    \
                 "global_load_lds_dwordx4 %1, %2 offset:2048 sc1\n\tglobal_load_lds_dwordx4 %1, %2 offset:3072 sc1\n\t" \
                 "s_mov_b32 m0, %6\n\ts_nop 0\n\tglobal_load_lds_dwordx4 %1, %3 sc1\n\t"                       \
                 "s_mov_b32 m0, %7\n\ts_nop 0\n\tglobal_load_lds_dwordx4 %1, %4 sc1\n\t"                       \
                 "s_mov_b32 m0, %0"                                                                            \
                 : "=&s"(sv_)                                                                                  \
                 : "v"(vl), "s"(src_ + wq * 5120), "s"(src_ + wq * 5120 + 4096), "s"(src_ + 20480), "s"(rfl(dst_ + wq * 5120)), \
                   "s"(rfl(dst_ + wq * 5120 + 4096)), "s"(rfl(dst_ + 20480))                                           \
                 : "memory");                                                                                  \
  } while (0)

#define AT_ISSUE2(C, SLOT)                                                                                     \
  do {                                                                                                         \
    const char* src_ = kvg + (size_t)(C) * AT_CH + wid * 10240;                                                \
    const unsigned dst_ = ldsb + (SLOT) + wid * 10240;                                                         \
    glds4_sc1(vl, vl, vl, vl, src_, rfl(dst_));                                                                \
    glds4_sc1(vl, vl, vl, vl, src_ + 4096, rfl(dst_ + 4096));                                                  \
    glds1_sc1(vl, src_ + 8192, rfl(dst_ + 8192)); glds1_sc1(vl, src_ + 9216, rfl(dst_ + 9216));                \
  } while (0)
  const char* pc;
  asm volatile("s_getpc_b64 %0" : "=s"(pc));
  pc = (const char*)((size_t)pc & ~(size_t)255);
#define CODE_TOUCH(IX, LANE, SINK) do { const int ln_ = (IX) * ((CODE_LINES + 31) / 32) + ((LANE) < (CODE_LINES + 31) / 32 ? (LANE) : 0); touch((ln_ < CODE_LINES ? ln_ : 0) * 128, pc, SINK); } while (0)
  if (wid == 7) CODE_TOUCH(idx, lane, ldsb + SCR_DUMMY);
  u32x4 rv0, rv1; u32x2 gep;
  asm volatile("global_load_dwordx4 %0, %3, %5\n\tglobal_load_dwordx4 %1, %4, %5 offset:1024\n\tglobal_load_dwordx2 %2, %6, %7 sc1"
               : "=&v"(rv0), "=&v"(rv1), "=&v"(gep) : "v"(vl), "v"(vl & 255), "s"(x + (size_t)(bid * 8 + wid) * 1280), "v"(0u), "s"(ws + WS_EP) : "memory");
  unsigned vx[4], vw[4];
#pragma unroll
  for (int i = 0; i < 4; ++i) {
    const int row = 8 * i + (lane >> 3), ch = (lane & 7) ^ ((4 * i + (lane >> 4)) & 7);
    vx[i] = row * 1280 + ch * 16 - 1024 * i;
    const int k = 4 * i + (lane >> 4), f = (lane >> 4) | ((i >> 1) << 2), cw = (lane & 15) ^ (f << 1);
    vw[i] = k * 3840 + cw * 16 - 1024 * i;
  }
  if (has_qkv) {
    if (wid < 4) {
      const char* p = x + (size_t)(rb * 128 + wid * 32) * 1280;
      glds4(vx[0], vx[1], vx[2], vx[3], p + sq0 * 128, ldsb + wid * 4096);
    } else {
      const char* p = wqkv + cb * 256 + (size_t)(wid - 4) * 16 * 3840;
      glds4(vw[0], vw[1], vw[2], vw[3], p + (size_t)sq0 * (64 * 3840), ldsb + WR + 49152 + (wid - 4) * 4096);
    }
  }
  {
    if (!has_qkv) asm volatile("s_waitcnt vmcnt(0)" ::: "memory");
    asm volatile("s_waitcnt vmcnt(4)" : "+v"(rv0), "+v"(rv1), "+v"(gep)::"memory");
    tag = make_tag(((u64)gep[1] << 32) | gep[0]);
    float s0 = 0.f, s1 = 0.f;
#pragma unroll
    for (int e = 0; e < 4; ++e) { s0 += bflo(rv0[e]) + bfhi(rv0[e]); s1 += bflo(rv1[e]) + bfhi(rv1[e]); }
    const float mean = sumx32(sumx16(sum16(lane < 16 ? s0 + s1 : s0))) * (1.0f / D);
    float q0 = 0.f, q1 = 0.f;
#pragma unroll
    for (int e = 0; e < 4; ++e) {
      const float a = bflo(rv0[e]) - mean, b = bfhi(rv0[e]) - mean, c = bflo(rv1[e]) - mean, d = bfhi(rv1[e]) - mean;
      q0 += a * a + b * b; q1 += c * c + d * d;
    }
    const float den = sqrtf(bfr(bfr(sumx32(sumx16(sum16(lane < 16 ? q0 + q1 : q0))) * (1.0f / D)) + 1e-5f));
    if (lane == 0) st_gran(ws + WS_ST + (size_t)(bid * 8 + wid) * 8, cvtpk(mean, den), tag);
  }
  if (wid < 2) {
    __builtin_amdgcn_s_setprio(3);
    unsigned vg[6];
#pragma unroll
    for (int j = 0; j < 6; ++j) {
      const int u = j * 64 + lane, k = u / 12, pos = u - k * 12;
      vg[j] = k * 3072 + (pos ^ (((k >> 3) & 1) * 2)) * 16 - 1024 * (j & 3);
    }
    const char* xb = x + (size_t)(rb * 128 + wid * 64) * 1280;
    const unsigned lx = ldsb + wid * 8192;
#define LX_ISSUE(ST, SO) do { glds4(vx[0], vx[1], vx[2], vx[3], xb + (ST) * 128, lx + (SO)); glds4(vx[0], vx[1], vx[2], vx[3], xb + (ST) * 128 + 32 * 1280, lx + 4096 + (SO)); } while (0)
    if (has_qkv) {
      const char* wb = wqkv + cb * 256 + (size_t)wid * 32 * 3840;
#define LQ_X(ST) do { kx = NEXT10(kx); LX_ISSUE(kx, ((ST) & 3) * 16384); } while (0)
#define LQ_W(ST)                                                                                                \
  do {                                                                                                          \
    kw = NEXT10(kw);                                                                                            \
    glds4(vw[0], vw[1], vw[2], vw[3], wb + (size_t)kw * (64 * 3840), lx + WR + (((ST) + 3) & 3) * 16384);           \
    glds4(vw[0], vw[1], vw[2], vw[3], wb + (size_t)kw * (64 * 3840) + 16 * 3840, lx + WR + 4096 + (((ST) + 3) & 3) * 16384); \
  } while (0)
      int kx = sq0, kw = sq0;
      LQ_X(1); LQ_W(1);
      asm volatile("" : "+v"(vg[0]), "+v"(vg[1]), "+v"(vg[2]), "+v"(vg[3]), "+v"(vg[4]), "+v"(vg[5]));
      WVB(16);
      LQ_X(2); LQ_W(2);
      WVB(16);
      LQ_X(3); LQ_W(3); LQ_X(4);
      WVB(24);
      LQ_W(4); LQ_X(5); LQ_W(5);
#pragma unroll 1
      for (int k = 3; k < 7; ++k) {
        WVB(32);
        LQ_X(k + 3); LQ_W(k + 3);
      }
#undef LQ_X
#undef LQ_W
    }
    const char* wb = (wid ? wu : wg) + fbk * 192;
    const unsigned lw = ldsb + wid * 12288;
#define LG_W(J, WO)                                                                                             \
  do {                                                                                                          \
    glds4(vg[0], vg[1], vg[2], vg[3], wb + (size_t)(J) * (64 * 3072), lw + (WO));                               \
    glds2(vg[4], vg[5], wb + (size_t)(J) * (64 * 3072), lw + 4096 + (WO));                                      \
    glds4(vg[0], vg[1], vg[2], vg[3], wb + (size_t)(J) * (64 * 3072) + 32 * 3072, lw + 6144 + (WO));            \
    glds2(vg[4], vg[5], wb + (size_t)(J) * (64 * 3072) + 32 * 3072, lw + 6144 + 4096 + (WO));                   \
  } while (0)
    int kw = sg0, kx = sg0;
    LG_W(kw, GWS);
    if (has_qkv) { WVB(44); WVB(28); }
    kw = NEXT10(kw); LG_W(kw, GWA);
    if (has_qkv) WVB(24);
    LX_ISSUE(kx, 32768); kx = NEXT10(kx);
    LX_ISSUE(kx, 49152); kx = NEXT10(kx);
    kw = NEXT10(kw); LG_W(kw, GWA + 24576);
    LX_ISSUE(kx, 0); kx = NEXT10(kx);
    WVB(28);
    LX_ISSUE(kx, 16384); kx = NEXT10(kx);
    int wo = GWS;
#pragma unroll 1
    for (int j = 3; j < 9; ++j) {
      WVB(28);
      kw = NEXT10(kw); LG_W(kw, wo);
      LX_ISSUE(kx, ((j + 3) & 3) * 16384); kx = NEXT10(kx);
      wo = wo == GWS ? GWA : wo + 24576;
    }
    WVB(28);
    kw = NEXT10(kw); LG_W(kw, GWS);
#undef LG_W
#undef LX_ISSUE
    WVB(20);
    {
      const char* qg = ws + WS_Q + (size_t)head * QHS + (size_t)(qb * 2 + wid) * QGS;
      glds4_sc1(vl, vl, vl, vl, qg, ldsb + SCR_Q + wid * 5120);
      glds1_sc1(vl, qg + 4096, ldsb + SCR_Q + wid * 5120 + 4096);
    }
    AT_ISSUE2(0, AT_A);
    AT_ISSUE2(1, AT_B);
    WVB(20);
    glds1_sc1(vl, kvg + (size_t)wid * AT_CH + 20480, rfl(ldsb + (wid ? AT_B : AT_A) + 20480));
  } else if (wid < 4) {
    const int hw = wid - 2, ht = hw * 64 + lane;
    const __amdgpu_buffer_rsrc_t rs = __builtin_amdgcn_make_buffer_rsrc(ws, 0, (int)WS_END, 0x00020000);
    __builtin_amdgcn_s_setprio(3);
#define HBAR() do { if (has_qkv) { SBAR(); BAR(); SBAR(); } } while (0)
    const bf16x8 ones = {0x3f80, 0x3f80, 0x3f80, 0x3f80, 0x3f80, 0x3f80, 0x3f80, 0x3f80};
    const int r = lane & 15, g = lane >> 4;
    const unsigned t32 = (unsigned)tag;
    char* csd = ws + WS_CS + (size_t)((cb * 16 + sq0) * 128 + hw * 64 + (r >> 2) * 16 + 4 * g + (r & 3)) * 8;
    char* cgd = ws + WS_CG + (size_t)((fbk * 16 + sg0) * 192 + hw * 96 + (r >> 2) * 16 + 4 * g + (r & 3)) * 8;
    unsigned sto = (unsigned)WS_ST + (rb * 128 + ht) * 8;
    unsigned cso = (unsigned)WS_CS + ((has_qkv ? cb : 0) * 16 + hh) * 1024 + hw * 512 + r32 * 16;
    unsigned cgo = (unsigned)WS_CG + fbk * 16 * 1536 + hw * 768 + (lane < 48 ? lane : 0) * 16;
    unsigned kpo = (unsigned)WS_KP + (head * 32 + r32) * 32 + hh * 16;
    const int c0 = (head * 20) >> 7, c1 = (head * 20 + 19) >> 7;
    const char* fkv = ws + WS_FQ + ((5 + (g >> 1) * 5 + ((g & 1) ? c1 : c0)) * 16 + r) * 8;
    const char* fq = ws + WS_FQ + (((lane & 1) ? c1 : c0) * 16 + qb * 2 + hw) * 8;
    lds_ptr wqa[4], wga[2];
#pragma unroll
    for (int nt = 0; nt < 4; ++nt) wqa[nt] = lds + WR + 49152 + (8 * g + (r >> 2)) * 256 + (((hw * 4 + nt) ^ ((r >> 2) | ((g & 1) << 2))) * 32) + (r & 3) * 8;
#pragma unroll
    for (int i = 0; i < 2; ++i) wga[i] = lds + GWS + hw * 12288 + (8 * g + (r >> 2)) * 192 + ((i ^ (g & 1)) * 32) + (r & 3) * 8;
    asm volatile("" : "+v"(csd), "+v"(cgd), "+v"(sto), "+v"(cso), "+v"(cgo), "+v"(kpo), "+v"(fkv), "+v"(fq), "+v"(wqa[0]), "+v"(wqa[1]), "+v"(wqa[2]), "+v"(wqa[3]),
                 "+v"(wga[0]), "+v"(wga[1]));
    if (has_qkv) {
      SBAR(); WVB(1); SBAR();
      f32x4 cs[4];
#pragma unroll
      for (int nt = 0; nt < 4; ++nt) {
        cs[nt] = MFMA16(cat8(ldstr(wqa[nt]), ldstr(wqa[nt] + 1024)), ones, (f32x4{0.f, 0.f, 0.f, 0.f}));
        cs[nt] = MFMA16(cat8(ldstr(wqa[nt] + 8192), ldstr(wqa[nt] + 9216)), ones, cs[nt]);
      }
      const f32x4 v = (r & 8) ? ((r & 4) ? cs[3] : cs[2]) : ((r & 4) ? cs[1] : cs[0]);
      st_gran(csd, f2u((r & 2) ? ((r & 1) ? v[3] : v[2]) : ((r & 1) ? v[1] : v[0])), tag);
      SBAR(); WLB(); SBAR();
      BAR(); BAR(); BAR(); BAR();
    }
    {
      u32x2 gst = bld8_sc1(rs, sto);
      u32x4 gc[5];
      if (has_qkv) {
#pragma unroll
        for (int i = 0; i < 5; ++i) gc[i] = bld16_sc1(rs, cso + i * 2048);
        BAR(); BAR();
      }
      _Pragma("unroll 1") for (unsigned spins = 0; spins < kSpinCap; ++spins) {
        unsigned bad = gst[1] ^ t32;
        if (has_qkv) {
#pragma unroll
          for (int i = 0; i < 5; ++i) bad |= (gc[i][1] ^ t32) | (gc[i][3] ^ t32);
        }
        if (wall(bad == 0u)) break;
        __builtin_amdgcn_s_sleep(2);
        asm volatile("" ::: "memory");
        gst = bld8_sc1(rs, sto);
        if (has_qkv) {
#pragma unroll
          for (int i = 0; i < 5; ++i) gc[i] = bld16_sc1(rs, cso + i * 2048);
        }
      }
      HBAR();
      *(LAS f32x2*)(lds + SCR_STAT + ht * 8) = f32x2{bflo(gst[0]), 1.0f / bfhi(gst[0])};
      if (has_qkv) {
        float a = 0.f, b = 0.f;
#pragma unroll
        for (int i = 0; i < 5; ++i) { a = adds(a, u2f(gc[i][0])); b = adds(b, u2f(gc[i][2])); }
        a = sumx32(a); b = sumx32(b);
        if (lane < 32) *(LAS f32x2*)(lds + SCR_CS + hw * 256 + lane * 8) = f32x2{a, b};
        WLB();
      }
    }
    BAR();
    {
      f32x4 cs[6];
#pragma unroll
      for (int nt = 0; nt < 6; ++nt) {
        const lds_ptr wa = wga[nt & 1] + (nt >> 1) * 64;
        cs[nt] = MFMA16(cat8(ldstr(wa), ldstr(wa + 768)), ones, (f32x4{0.f, 0.f, 0.f, 0.f}));
        cs[nt] = MFMA16(cat8(ldstr(wa + 6144), ldstr(wa + 6912)), ones, cs[nt]);
      }
      asm volatile("" : "+v"(cs[0]), "+v"(cs[1]), "+v"(cs[2]), "+v"(cs[3]), "+v"(cs[4]), "+v"(cs[5]));
      SBAR(); WLB(); SBAR();
      const f32x4 v = (r & 8) ? ((r & 4) ? cs[3] : cs[2]) : ((r & 4) ? cs[1] : cs[0]), w = (r & 4) ? cs[5] : cs[4];
      st_gran(cgd, f2u((r & 2) ? ((r & 1) ? v[3] : v[2]) : ((r & 1) ? v[1] : v[0])), tag);
      if (r < 8) st_gran(cgd + 512, f2u((r & 2) ? ((r & 1) ? w[3] : w[2]) : ((r & 1) ? w[1] : w[0])), tag);
    }
    BAR();
    if (ht == 0 && has_qkv) set_flag(ws + WS_FQ + (cb * 16 + rb) * 8, tag);
    BAR();
    {
      u32x4 gc[10];
#pragma unroll
      for (int i = 0; i < 10; ++i) gc[i] = bld16_sc1(rs, cgo + i * 1536);
      BAR(); BAR();
      _Pragma("unroll 1") for (unsigned spins = 0; spins < kSpinCap; ++spins) {
        unsigned bad = 0u;
#pragma unroll
        for (int i = 0; i < 10; ++i) bad |= (gc[i][1] ^ t32) | (gc[i][3] ^ t32);
        if (wall(bad == 0u)) break;
        __builtin_amdgcn_s_sleep(2);
        asm volatile("" ::: "memory");
#pragma unroll
        for (int i = 0; i < 10; ++i) gc[i] = bld16_sc1(rs, cgo + i * 1536);
      }
      SBAR(); BAR(); SBAR();
      const u64 f0 = __hip_atomic_load((gu64*)fkv, RLX_AGENT), f1 = __hip_atomic_load((gu64*)fq, RLX_AGENT);
      float a = 0.f, b = 0.f;
#pragma unroll
      for (int i = 0; i < 10; ++i) { a = adds(a, u2f(gc[i][0])); b = adds(b, u2f(gc[i][2])); }
      if (lane < 48) *(LAS f32x2*)(lds + SCR_CG + hw * 384 + lane * 8) = f32x2{a, b};
      SBAR(); BAR(); SBAR();
      if (!wall(f0 == tag && f1 == tag)) wait_tag(fkv, fq, tag);
      __builtin_amdgcn_fence(__ATOMIC_ACQUIRE, "wavefront");
    }
    {
      const u32x4 kp = bld16_sc1(rs, kpo);
      BAR();
      const float ks = hh ? u2f(kp[0]) : adds(adds(u2f(kp[0]), u2f(kp[1])), adds(u2f(kp[2]), u2f(kp[3])));
      const float kn2 = maxx16(max16(sumx32(ks)));
      if (ht == 0) *(LAS float*)(lds + SCR_KN) = kn2;
      WLB();
    }
  } else {
    const int cw = wid - 4;
    __builtin_amdgcn_s_setprio(2);
    Pend pd = {};
    pd.base = ws;
    if (has_qkv) {
      if (cb < 10) qkv_compute<true>(lds, cw, lane, rb, cb, ws, pd);
      else qkv_compute<false>(lds, cw, lane, rb, cb, ws, pd);
    }
    gu_compute(lds, cw, lane, rb, fbk, ws, pd, has_qkv);
    __builtin_amdgcn_s_setprio(0);
  }
  {
    const lds_ptr qp = lds + SCR_Q + ((wid >> 1) & 1) * 5120 + (wid & 1) * 512 + r32 * 16;
    const u32x4 l0 = __builtin_bit_cast(u32x4, lds128(qp + hh * 2048)), l1 = __builtin_bit_cast(u32x4, lds128(qp + hh * 2048 + 1024));
    const u32x4 qbb = __builtin_bit_cast(u32x4, lds128(qp + 4096));
#pragma unroll
    for (int a = 0; a < 2; ++a) {
      qa[a] = __builtin_bit_cast(bf16x8, u32x4{l0[2 * a], l0[2 * a + 1], l1[2 * a], l1[2 * a + 1]});
#pragma unroll
      for (int j = 0; j < 2; ++j) { u32x2 u = {hh == j ? qbb[2 * a] : 0u, hh == j ? qbb[2 * a + 1] : 0u}; qv[a][j] = __builtin_bit_cast(s16x4, u); }
    }
    LAS f32x2* mp = (LAS f32x2*)(lds + SCR_M + (wid & 3) * 256 + r32 * 8);
    if (wid < 4) {
      const float kn2 = *(const LAS float*)(lds + SCR_KN);
#pragma unroll
      for (int a = 0; a < 2; ++a) {
        const unsigned w[5] = {hh ? qbb[2 * a + 1] : qbb[2 * a], l0[2 * a], l0[2 * a + 1], l1[2 * a], l1[2 * a + 1]};
        float ss = 0.f;
#pragma unroll
        for (int e = 0; e < 5; ++e) { ss = fmas(bflo(w[e]), bflo(w[e]), ss); ss = fmas(bfhi(w[e]), bfhi(w[e]), ss); }
        mrow[a] = __builtin_amdgcn_sqrtf(fmas(sumx32(ss), kn2 * 1.04f, 0.f));
      }
      *mp = f32x2{mrow[0], mrow[1]};
      WVB(0);
      __builtin_amdgcn_s_setprio(0);
    } else {
      const f32x2 m = *mp;
      mrow[0] = m[0]; mrow[1] = m[1];
    }
  }

  const int kh = wid >> 2;
#define FRESH()                                                                                                \
  int lane_ = lane, bid_ = bid, wq = wid & 3; char* wz = ws;                                                   \
  asm volatile("" : "+v"(lane_));                                                                              \
  asm volatile("" : "+s"(bid_), "+s"(wz), "+s"(wq));                                                           \
  const int xc = bid_ & 7, ix = bid_ >> 3, mb = (xc >> 1) * 8 + (ix & 7), nb = (xc & 1) * 4 + (ix >> 3);       \
  const unsigned vl = lane_ * 16;                                                                              \
  const char* kvg = wz + WS_KV + (size_t)(xc * 4 + (ix >> 3)) * KVHS;                                          \
  (void)vl; (void)mb; (void)nb; (void)kvg
  {
    const int dsel = r32 <= 20 ? r32 : 21;
    const int koff = kh * (int)KVGS + hh * 2048 + r32 * 16, kboff = kh * (int)KVGS + 4096 + lane * 16, vsw = r32 < 28 ? (dsel >> 2) & 3 : 3;
    const int voff0 = kh * (int)KVGS + 5120 + dsel * 64 + ((hh ^ vsw) * 16), voff1 = voff0 ^ 32;
    bool pub = false;
    for (int pass = 0;; ++pass) {
      f32x16 negm[2], sc[2], oacc[2];
#pragma unroll
      for (int a = 0; a < 2; ++a)
#pragma unroll
        for (int e = 0; e < 16; ++e) { negm[a][e] = -mrow[a]; oacc[a][e] = 0.f; }
      asm volatile("" : "+v"(negm[0]), "+v"(negm[1]));
      u32x4 pw[2][2] = {};
      bf16x8 ka, kb, vf[2] = {};
      int sl_cur = AT_A, sl_next = AT_B;
      unsigned hook = wid < 4 ? 0x0fu : 0x5au;
      u64 tbl = (u64)(AT_CH >> 8) | (u64)(2 * AT_CH >> 8) << 10 | (u64)(3 * AT_CH >> 8) << 20 | (u64)(AT_A >> 8) << 30 | (u64)(AT_6 >> 8) << 40 | (u64)(AT_7 >> 8) << 50;
      asm volatile("" : "+s"(hook), "+s"(tbl));
#define LDKA(P, t) cat8(lds64((P) + ((t) >> 1) * 512 + ((t) & 1) * 8), lds64((P) + 1024 + ((t) >> 1) * 512 + ((t) & 1) * 8))
      ka = LDKA(lds + koff + AT_A, 0);
      kb = lds128(lds + kboff + AT_A);
#define KBH(t) (((t) & 1) ? __builtin_shufflevector(kb, kb, 4, 5, 6, 7) : __builtin_shufflevector(kb, kb, 0, 1, 2, 3))
      sc[0] = MFMA32(ka, qa[0], negm[0]);
      sc[0] = MFMA32K8(KBH(0), qv[0][0], sc[0]);
#define EX4(X, i) do { X[i] = __builtin_amdgcn_exp2f(X[i]); X[i + 1] = __builtin_amdgcn_exp2f(X[i + 1]); X[i + 2] = __builtin_amdgcn_exp2f(X[i + 2]); X[i + 3] = __builtin_amdgcn_exp2f(X[i + 3]); } while (0)
#define PK2(P, j, X, i) do { P[j] = cvtpk(X[i], X[i + 1]); P[j + 1] = cvtpk(X[i + 2], X[i + 3]); } while (0)
#define PFR(a, s) __builtin_bit_cast(bf16x8, pw[a][s])
#define EX2(X, i) do { X[i] = __builtin_amdgcn_exp2f(X[i]); X[i + 1] = __builtin_amdgcn_exp2f(X[i + 1]); } while (0)
#define UNIT(A, B, TB, LDK, LDV)                                                             \
  do {                                                                                       \
    sc[B] = MFMA32(ka, qa[B], negm[B]); SBAR();                                              \
    EX4(sc[A], 0); EX2(sc[A], 4); PIN(sc[A]); SBAR();                                        \
    sc[B] = MFMA32K8(KBH(TB), qv[B][(TB) >> 1], sc[B]); SBAR();                              \
    LDK; SBAR();                                                                             \
    EX2(sc[A], 6); SBAR();                                                                   \
    PK2(pw[A][0], 0, sc[A], 0); PK2(pw[A][0], 2, sc[A], 4); PIN(sc[A]); PIN(pw[A][0]); SBAR(); \
    oacc[B] = MFMA32(vf[0], PFR(B, 0), oacc[B]); SBAR();                                     \
    EX4(sc[A], 8); EX2(sc[A], 12); PIN(sc[A]); SBAR();                                       \
    oacc[B] = MFMA32(vf[1], PFR(B, 1), oacc[B]); SBAR();                                     \
    LDV; SBAR();                                                                             \
    EX2(sc[A], 14); SBAR();                                                                  \
    PK2(pw[A][1], 0, sc[A], 8); PK2(pw[A][1], 2, sc[A], 12); PIN(pw[A][1]); SBAR();          \
  } while (0)
#define LDV_(t) do { vf[0] = lds128(vp0 + (t) * 1408); vf[1] = lds128(vp1 + (t) * 1408); } while (0)
      for (int c = 0; c < 8; ++c) {
        lds_ptr kp_ = lds + koff + sl_cur, vp0 = lds + voff0 + sl_cur, vp1 = lds + voff1 + sl_cur, kn = lds + koff + sl_next, kbn = lds + kboff + sl_next;
        UNIT(0, 1, 0, ka = LDKA(kp_, 1), LDV_(0));
        UNIT(1, 0, 1, (void)0, (void)0);
        UNIT(0, 1, 1, ka = LDKA(kp_, 2), LDV_(1));
        UNIT(1, 0, 2, (void)0, (void)0);
        UNIT(0, 1, 2, ka = LDKA(kp_, 3), LDV_(2));
        UNIT(1, 0, 3, (void)0, (void)0);
        if (__builtin_expect((hook >> c) & 1, 0)) {
         if (c & 1) WVB(0);
         if (wid >= 4) {
          if (c == 3) __builtin_amdgcn_s_setprio(1);
          else if (c >= 4) {
            FRESH();
            const unsigned lt = (wq * 64 + lane_) * 128, sink = rfl(ldsb + AT_SY + 256);
            if (c == 4) {
              wait_tag(wz + WS_FH + ((xc >> 1) * 64 + lane_) * 8, tag);
              const char* hp = wz + WS_H + (size_t)((xc >> 1) * 32 + ix) * 49152;
              touch_sc1(lt, hp, sink);
              if (wq < 2) touch_sc1(lt, hp + 32768, sink);
              else touch_sc1(lane_ * 1280 + (wq - 2) * 156, x + (size_t)mb * 64 * 1280 + nb * 160, sink);
            } else {
              wait_tag(wz + WS_FW + ((xc & 1) * 128 + lane_) * 8, wz + WS_FW + ((xc & 1) * 128 + 64 + (lane_ & 3)) * 8, tag);
              const char* wp = wz + WS_WP + (size_t)(xc & 1) * 4 * 68 * 5120 + (size_t)ix * 43520;
              touch_sc1(lt, wp, sink);
              if (wq < 2) touch_sc1(lt < 43520 - 32768 ? lt : 0u, wp + 32768, sink);
              if (wq == 3) CODE_TOUCH(ix, lane_, sink);
            }
          }
         } else {
          FRESH();
          const int rl_ = (xc & 1) ? ix - 15 : ix, rj = xc * 17 + rl_;
          const bool rp = pass == 0 && rl_ >= 0 && rl_ < 17;
          if (c == 0) {
            AT_ISSUE(2, AT_CH); AT_ISSUE(3, 2 * AT_CH);
            if (wq == 0 && lane_ < 16) *(LAS unsigned*)(lds + AT_SY + lane_ * 4) = 0u;
        if (rp) {
          const int ks = rj >> 1;
          unsigned v[5];
#pragma unroll
          for (int j = 0; j < 5; ++j) {
            const int u = j * 64 + lane_, pr = u / 10, pos = u - pr * 10, k = pr ^ (((pr >> 3) & 1) << 2);
            const int row = ks < 48 ? k : ks < 64 ? (k >> 4) * 20 + (k & 15) : (k >> 2) * 20 + 16 + (k & 3);
            v[j] = row * 1280 + pos * 16 - 1024 * (j & 3);
          }
          const char* src = ks < 48 ? wd + (size_t)ks * 32 * 1280 : ks < 64 ? wo + (size_t)(ks - 48) * 40 * 1280 : wo + (size_t)(ks - 64) * 160 * 1280;
          glds5(v, rflp(src + ((rj & 1) * 4 + wq) * 160), rfl(ldsb + AT_RP + wq * 5120));
        }
          } else if (c == 1) {
            AT_ISSUE(4, 3 * AT_CH); AT_ISSUE(5, AT_A);
            if (pass == 0) {
              if (wq == 0 && lane_ == 0) {
                const int t_ = (xc & 1) * 30 + ix;
                set_flag(wz + WS_FH + (((xc >> 1) * 4 + (t_ & 3)) * 16 + (xc & 1) * 8 + (ix >> 2)) * 8, tag);
              }
              if (rp) {
                u32x4 t[5];
#pragma unroll
                for (int j = 0; j < 5; ++j) {
                  const int r = lane_ & 15, g = lane_ >> 4;
                  const lds_ptr w0 = lds + AT_RP + wq * 5120 + (8 * g + ((r >> 2) ^ (4 * (g & 1)))) * 160 + (r & 3) * 8 + j * 32;
                  const lds_ptr w1 = lds + AT_RP + wq * 5120 + (8 * g + ((4 + (r >> 2)) ^ (4 * (g & 1)))) * 160 + (r & 3) * 8 + j * 32;
                  t[j] = __builtin_bit_cast(u32x4, cat8(ldstr(w0), ldstr(w1)));
                }
#pragma unroll
                for (int j = 0; j < 5; ++j) st16_sc1(wz + WS_WP + (size_t)(((rj & 1) * 4 + wq) * 68 + (rj >> 1)) * 5120, vl + j * 1024, t[j]);
              }
            }
          } else if (c == 2) {
            AT_ISSUE(6, AT_6); AT_ISSUE(7, AT_7);
          } else {
            if (rp && wq == 0 && lane_ == 0) set_flag(wz + WS_FW + ((rj & 1) * 128 + (rj >> 1)) * 8, tag);
          }
         }
        }
        SBAR();
        UNIT(0, 1, 3, do { ka = LDKA(kn, 0); kb = lds128(kbn); } while (0), LDV_(3));
        UNIT(1, 0, 0, (void)0, (void)0);
        sl_cur = sl_next;
        sl_next = ((unsigned)tbl & 1023u) << 8;
        tbl >>= 10;
      }
      oacc[1] = MFMA32(vf[0], PFR(1, 0), oacc[1]);
      oacc[1] = MFMA32(vf[1], PFR(1, 1), oacc[1]);
      f32x4 o2; float lpart;
#pragma unroll
      for (int e = 0; e < 4; ++e) {
        auto sw = __builtin_amdgcn_permlane32_swap(f2u(oacc[0][8 + e]), f2u(oacc[1][8 + e]), false, false);
        o2[e] = u2f(sw[0]);
        if (e == 0) lpart = u2f(sw[1]);
      }
      {
        FRESH();
        volatile LAS unsigned* sy = (volatile LAS unsigned*)(lds + AT_SY);
        const lds_ptr ex = lds + AT_EX + wq * 5376;
        if (kh) {
#pragma unroll
          for (int i = 0; i < 4; ++i) *(LAS f32x4*)(ex + i * 1024 + vl) = f32x4{oacc[i >> 1][4 * (i & 1)], oacc[i >> 1][4 * (i & 1) + 1], oacc[i >> 1][4 * (i & 1) + 2], oacc[i >> 1][4 * (i & 1) + 3]};
          *(LAS f32x4*)(ex + 4096 + vl) = o2; *(LAS float*)(ex + 5120 + lane_ * 4) = lpart;
          asm volatile("s_waitcnt vmcnt(0) lgkmcnt(0)" ::: "memory");
          if (lane_ == 0) sy[8 + wq] = 1u;
          if (bid_ == 255 && wq == 0) {
#pragma unroll 1
            for (int i = 0; i < 2; ++i) wait_tag(wz + WS_FH + (i * 128 + lane_) * 8, wz + WS_FH + (i * 128 + 64 + lane_) * 8, tag);
            if (lane_ == 0) set_flag(wz + WS_EP, tag);
          }
          const __amdgpu_buffer_rsrc_t rs = __builtin_amdgcn_make_buffer_rsrc(wz, 0, (int)WS_END, 0x00020000);
          unsigned xo[4];
#pragma unroll
          for (int i = 0; i < 4; ++i) xo[i] = (unsigned)WS_H + (mb * 4 + (i ^ wq)) * 49152 + wq * 1024 + vl;
          const unsigned wo_ = (unsigned)WS_WP + (nb * 68 + wq) * 5120 + vl;
          int sn = ((ix >> 3) * 3 + (ix & 7)) % 12;
          f32x4 ac4[5][4];
#pragma unroll
          for (int i = 0; i < 5; ++i)
#pragma unroll
            for (int j = 0; j < 4; ++j) ac4[i][j] = f32x4{0.f, 0.f, 0.f, 0.f};
          u32x4 hx[3][4], hw_[3][5];
#define H_LOAD(B)                                                                                              \
  do {                                                                                                         \
    _Pragma("unroll") for (int i = 0; i < 5; ++i) {                                                            \
      hw_[B][i] = __builtin_amdgcn_raw_buffer_load_b128(rs, wo_ + i * 1024, sn * 20480, 16);                   \
      if (i < 4) hx[B][i] = __builtin_amdgcn_raw_buffer_load_b128(rs, xo[i], sn * 4096, 16);                   \
    }                                                                                                          \
    sn = sn == 11 ? 0 : sn + 1;                                                                                \
  } while (0)
#define H_MMA(B)                                                                                               \
  do {                                                                                                         \
    _Pragma("unroll") for (int nt = 0; nt < 5; ++nt)                                                           \
      _Pragma("unroll") for (int mt = 0; mt < 4; ++mt)                                                         \
        ac4[nt][mt] = MFMA16(__builtin_bit_cast(bf16x8, hw_[B][nt]), __builtin_bit_cast(bf16x8, hx[B][mt]), ac4[nt][mt]); \
  } while (0)
          H_LOAD(0); H_LOAD(1); H_LOAD(2);
#pragma unroll 1
          for (int it = 0; it < 3; ++it) {
            SBAR(); H_MMA(0); SBAR(); H_LOAD(0);
            SBAR(); H_MMA(1); SBAR(); H_LOAD(1);
            SBAR(); H_MMA(2); SBAR(); H_LOAD(2);
            if (it == 1) __builtin_amdgcn_s_setprio(0);
          }
          SBAR(); H_MMA(0); H_MMA(1); H_MMA(2);
          f32x4 acc[5], acs[5];
          _Pragma("unroll 1") for (unsigned spins = 0; spins < (kSpinCap << 6); ++spins) {
            if (rfl(read_sy(lds + D_SY + 32))) break;
            __builtin_amdgcn_s_sleep(1);
          }
#pragma unroll
          for (int nt = 0; nt < 5; ++nt) {
            *(LAS f32x4*)(lds + D_PS + (wq ^ 2) * 10240 + nt * 1024 + vl) = ac4[nt][2];
            *(LAS f32x4*)(lds + D_PS + (wq ^ 2) * 10240 + 5120 + nt * 1024 + vl) = ac4[nt][3];
            acc[nt] = ac4[nt][0]; acs[nt] = ac4[nt][1];
          }
          if (rfl(read_sy(lds + D_SY))) { WLB(); }
          else {
    const int r = lane_ & 15, g = lane_ >> 4;
    const unsigned xo0 = (unsigned)WS_O + (mb * 4 + wq) * 20480 + (wq & 1) * 1024 + vl, xo1 = (unsigned)WS_O + (mb * 4 + (wq ^ 1)) * 20480 + (wq & 1) * 1024 + vl;
    const lds_ptr wa = lds + (wq & 1) * 5120 + vl;
    const int j0 = ((ix >> 3) * 5 + 1) >> 1;
    SBAR(); WLB(); SBAR();
    bf16x8 wf[2][5];
    u32x2 xres[5];
#pragma unroll
    for (int nt = 0; nt < 5; ++nt) {
      wf[0][nt] = lds128(wa + j0 * 10240 + nt * 1024);
      acc[nt] += *(const LAS f32x4*)(lds + D_PS + wq * 10240 + nt * 1024 + vl);
      acs[nt] += *(const LAS f32x4*)(lds + D_PS + wq * 10240 + 5120 + nt * 1024 + vl);
      xres[nt] = *(const LAS u32x2*)(lds + SCR + 4096 + wq * 3072 + r * 160 + g * 8 + nt * 32);
      acc[nt] += f32x4{bflo(xres[nt][0]), bfhi(xres[nt][0]), bflo(xres[nt][1]), bfhi(xres[nt][1])};
      PIN(acc[nt]); PIN(acs[nt]); PIN(wf[0][nt]);
    }
    SBAR(); BAR(); SBAR();
    u32x4 ox[10][2];
    int jj = j0;
#define O_LOAD(J) do { ox[J][0] = __builtin_amdgcn_raw_buffer_load_b128(rs, xo0, jj * 2048, 16); ox[J][1] = __builtin_amdgcn_raw_buffer_load_b128(rs, xo1, jj * 2048, 16); jj = NEXT10(jj); } while (0)
#pragma unroll
    for (int j = 0; j < 5; ++j) O_LOAD(j);
    SBAR();
    int jn = j0;
#pragma unroll
    for (int j = 0; j < 10; ++j) {
      jn = NEXT10(jn);
      if (j + 5 < 10) O_LOAD(j + 5);
      if (j < 9) {
#pragma unroll
        for (int nt = 0; nt < 5; ++nt) wf[(j + 1) & 1][nt] = lds128(wa + jn * 10240 + nt * 1024);
      }
#pragma unroll
      for (int nt = 0; nt < 5; ++nt) {
        acs[nt] = MFMA16(wf[j & 1][nt], __builtin_bit_cast(bf16x8, ox[j][1]), acs[nt]);
        acc[nt] = MFMA16(wf[j & 1][nt], __builtin_bit_cast(bf16x8, ox[j][0]), acc[nt]);
      }
      SBAR();
    }
#pragma unroll
    for (int nt = 0; nt < 5; ++nt) *(LAS f32x4*)(lds + D_PS + wq * 10240 + nt * 1024 + vl) = acs[nt];
    WLB();
#pragma unroll
    for (int nt = 0; nt < 5; ++nt) acc[nt] += *(const LAS f32x4*)(lds + D_PS + (wq ^ 1) * 10240 + nt * 1024 + vl);
    unsigned w[5][2];
#pragma unroll
    for (int nt = 0; nt < 5; ++nt) {
      w[nt][0] = cvtpk(acc[nt][0], acc[nt][1]);
      w[nt][1] = cvtpk(acc[nt][2], acc[nt][3]);
    }
    char* op = out + ((size_t)((mb * 64 + wq * 16 + r) * 640 + nb * 80)) * 2;
#pragma unroll
    for (int j = 0; j < 2; ++j) {
      auto s0 = __builtin_amdgcn_permlane16_swap(w[2 * j][0], w[2 * j + 1][0], false, false);
      auto s1 = __builtin_amdgcn_permlane16_swap(w[2 * j][1], w[2 * j + 1][1], false, false);
      *(u32x4*)(op + j * 64 + (g & 1) * 32 + (g >> 1) * 16) = u32x4{s0[0], s1[0], s0[1], s1[1]};
    }
    *(u32x2*)(op + 128 + g * 8) = u32x2{w[4][0], w[4][1]};
            return;
          }
        } else {
          __builtin_amdgcn_s_setprio(3);
          _Pragma("unroll 1") for (unsigned spins = 0; spins < (kSpinCap << 6); ++spins) {
            if (rfl(sy[8 + wq])) break;
            __builtin_amdgcn_s_sleep(1);
          }
          asm volatile("" ::: "memory");
          f32x4 p[5];
#pragma unroll
          for (int i = 0; i < 5; ++i) p[i] = *(const LAS f32x4*)(ex + i * 1024 + vl);
          const float lsum = adds(lpart, *(const LAS float*)(ex + 5120 + lane_ * 4));
          const bool bad = pass == 0 && wany(!(lsum >= 0x1p-70f && lsum <= 0x1p40f));
          const int head = xc * 4 + (ix >> 3), qb = ix & 7, r32 = lane_ & 31, hh = lane_ >> 5;
          const float rl = __builtin_amdgcn_rcpf(lsum);
          auto sr = __builtin_amdgcn_permlane32_swap(f2u(rl), f2u(rl), false, false);
#pragma unroll
          for (int a = 0; a < 2; ++a) {
            const float ra = u2f(sr[a]);
            unsigned w[4];
#pragma unroll
            for (int i = 0; i < 4; ++i)
              w[i] = cvtpk(muls(adds(oacc[a][2 * i], p[2 * a + (i >> 1)][2 * (i & 1)]), ra), muls(adds(oacc[a][2 * i + 1], p[2 * a + (i >> 1)][2 * (i & 1) + 1]), ra));
            auto s0 = __builtin_amdgcn_permlane32_swap(w[0], w[2], false, false);
            auto s1 = __builtin_amdgcn_permlane32_swap(w[1], w[3], false, false);
            const int tok = qb * 256 + wq * 64 + a * 32 + r32;
            if (!pub) st16_sc1(wz + WS_O, (((tok >> 4) * 80 + head * 2 + hh) * 16 + (tok & 15)) * 16, u32x4{s0[0], s1[0], s0[1], s1[1]});
          }
          const int tok = qb * 256 + wq * 64 + lane_;
          if (!pub) st8_sc1(wz + WS_O + ((size_t)((tok >> 4) * 80 + 64 + (head >> 1)) * 16 + (tok & 15)) * 16 + (head & 1) * 8,
                  u32x2{cvtpk(muls(adds(o2[0], p[4][0]), rl), muls(adds(o2[1], p[4][1]), rl)), cvtpk(muls(adds(o2[2], p[4][2]), rl), muls(adds(o2[3], p[4][3]), rl))});
          pub = !bad;
          unsigned cnt = 0;
          if (lane_ == 0) cnt = __hip_atomic_fetch_add((LAS unsigned*)(lds + AT_SY + 48), bad ? 0x101u : 1u, __ATOMIC_RELAXED, __HIP_MEMORY_SCOPE_WORKGROUP) + (bad ? 0x101u : 1u);
          cnt = rfl(cnt);
          if ((cnt & 255u) == 4u && lane_ == 0) {
            *(volatile LAS unsigned*)(lds + D_SY) = cnt >> 8;
            *(volatile LAS unsigned*)(lds + D_SY + 32) = 1u;
          }
          const char* wp = wz + WS_WP + (size_t)(nb * 68 + 48) * 5120 + wq * 25600;
          asm volatile("s_waitcnt vmcnt(0)" ::: "memory");
          if (!bad && lane_ == 0) set_flag(wz + WS_FO + ((qb * 4 + wq) * 32 + head) * 8, tag);
          _Pragma("unroll 1") for (unsigned spins = 0; spins < (kSpinCap << 6); ++spins) {
            if (rfl(read_sy(lds + D_SY + 32))) break;
            __builtin_amdgcn_s_sleep(1);
          }
#pragma unroll
          for (int i = 0; i < 6; ++i) glds4_sc1(vl, vl, vl, vl, wp + i * 4096, rfl(ldsb + wq * 25600 + i * 4096));
          glds1_sc1(vl, wp + 24576, rfl(ldsb + wq * 25600 + 24576));
          {
            unsigned v[3];
#pragma unroll
            for (int j = 0; j < 3; ++j) { const int u = min(j * 64 + lane_, 159), row = u / 10; v[j] = row * 1280 + (u - row * 10) * 16; }
            const char* xp = x + (size_t)(mb * 64 + wq * 16) * 1280 + nb * 160;
            glds2(v[0], v[1] - 1024, xp, rfl(ldsb + SCR + 4096 + wq * 3072));
            glds1_sc1(v[2], xp, rfl(ldsb + SCR + 4096 + wq * 3072 + 2048));
          }
          WVB(0);
          if (!rfl(read_sy(lds + D_SY))) {
            if (wq == 0) {
              const char* fp = wz + WS_FO + mb * 256 + (lane_ & 31) * 8;
              wait_tag<4>(fp, tag);
              BAR();
            }
            return;
          }
        }
      }
      FRESH();
      __builtin_amdgcn_s_setprio(0);
      WVB(0);
      if (wid < 4) { AT_ISSUE(0, AT_A); AT_ISSUE(1, AT_B); }
      float mx[2] = {-INFINITY, -INFINITY};
      int lane = threadIdx.x & 63;
      PIN(lane);
      const int r32 = lane & 31, hh = lane >> 5;
      for (int gi = 0; gi < 16; ++gi) {
        const char* gp = kvg + (size_t)gi * KVGS;
        kb = __builtin_bit_cast(bf16x8, ld16_sc1_wait(gp + 4096 + lane * 16));
#pragma unroll
        for (int tp = 0; tp < 2; ++tp) {
          const u32x4 k0 = ld16_sc1_wait(gp + hh * 2048 + r32 * 16 + tp * 512), k1 = ld16_sc1_wait(gp + hh * 2048 + r32 * 16 + 1024 + tp * 512);
#pragma unroll
          for (int u = 0; u < 2; ++u) {
            ka = __builtin_bit_cast(bf16x8, u32x4{k0[2 * u], k0[2 * u + 1], k1[2 * u], k1[2 * u + 1]});
#pragma unroll
            for (int a = 0; a < 2; ++a) {
              f32x16 s = MFMA32(ka, qa[a], f32x16{});
              s = MFMA32K8(KBH(u), qv[a][tp], s);
#pragma unroll
              for (int e = 0; e < 16; ++e) mx[a] = fmaxf(mx[a], s[e]);
            }
          }
        }
      }
#pragma unroll
      for (int a = 0; a < 2; ++a) mrow[a] = maxx32(mx[a]);
      WVB(0);
    }
  }
#undef AT_ISSUE
#undef UNIT

}

extern "C" void kernel_launch(void* const* d_in, const int* in_sizes, int n_in, void* d_out, int out_size, void* d_ws, size_t ws_size,
                              hipStream_t stream) {
  fused_block<<<256, 512, 0, stream>>>((const char*)d_in[0], (const char*)d_in[1], (const char*)d_in[2], (const char*)d_in[4],
                                       (const char*)d_in[5], (const char*)d_in[6], (char*)d_ws, (char*)d_out);
}
```
